```python
import torch

def _softmax(x, axis=-1):
    return torch.softmax(x, dim=axis)

def _gelu(x):
    return torch.nn.functional.gelu(x, approximate='tanh')

def _silu(x):
    return torch.nn.functional.silu(x)

def _sigmoid(x):
    return torch.sigmoid(x)

def _layernorm(x, axis=-1, eps=1e-05):
    m = x.mean(dim=axis, keepdim=True)
    v = x.var(dim=axis, keepdim=True, unbiased=False)
    return (x - m) / torch.sqrt(v + eps)

def _scatter_add(tgt, idx, src, axis=0):
    return tgt.clone().index_add_(axis, idx, src)

def _segment_sum(src, seg, n):
    out = torch.zeros((n,) + src.shape[1:], dtype=src.dtype)
    return out.index_add_(0, seg, src)

def _pad(x, pw):
    flat = tuple((v for pair in reversed(pw) for v in pair))
    return torch.nn.functional.pad(x, flat)
kernel_name = 'gated_mix__sliding_block__memory_aug_recurrent_mem_tr__prompt'

def setup_inputs():

    _w = lambda a: (lambda t: t.to(torch.bfloat16) if t.is_floating_point() else t)(torch.as_tensor(a))

    def _A_setup():
        B, S, D = (1, 2048, 512)
        return {'x': _w((lambda *s: torch.randn(*s, dtype=torch.bfloat16) * 0.05)(B, S, D)), 'wqkv': _w((lambda *s: torch.randn(*s, dtype=torch.bfloat16) * 1.6)(D, 3 * D)), 'wo': _w((lambda *s: torch.randn(*s, dtype=torch.bfloat16) * 0.05)(D, D))}
    _A_in = _A_setup()

    def _B_setup():
        B, S, Dm = (1, 2048, 512)
        H, Dh = (32, 128)
        M, seg = (8, 32)
        seg = min(seg, S)
        S = S // seg * seg
        return {'x': _w((lambda *s: torch.randn(*s, dtype=torch.bfloat16) * 0.05)(B, S, Dm)), 'mem0': _w((lambda *s: torch.randn(*s, dtype=torch.bfloat16) * 1.6)(B, M, Dm)), 'Wq': _w((lambda *s: torch.randn(*s, dtype=torch.bfloat16) * 0.8)(Dm, H, Dh) * Dm ** (-0.5)), 'Wk': _w((lambda *s: torch.randn(*s, dtype=torch.bfloat16) * 0.8)(Dm, H, Dh) * Dm ** (-0.5)), 'Wv': _w((lambda *s: torch.randn(*s, dtype=torch.bfloat16) * 1.6)(Dm, H, Dh) * Dm ** (-0.5)), 'Wo': _w((lambda *s: torch.randn(*s, dtype=torch.bfloat16) * 1.6)(H, Dh, Dm) * (H * Dh) ** (-0.5)), 'seg_len': seg}
    _B_in = _B_setup()
    return {'A_x': _A_in['x'], 'A_wqkv': _A_in['wqkv'], 'A_wo': _A_in['wo'], 'B_x': _B_in['x'], 'B_mem0': _B_in['mem0'], 'B_Wq': _B_in['Wq'], 'B_Wk': _B_in['Wk'], 'B_Wv': _B_in['Wv'], 'B_Wo': _B_in['Wo'], 'B_seg_len': _B_in['seg_len'], 'gate_w': _w((lambda *s: torch.randn(*s, dtype=torch.bfloat16) * 0.05)(512) * 16.0)}

def reference(A_x, A_wqkv, A_wo, B_x, B_mem0, B_Wq, B_Wk, B_Wv, B_Wo, B_seg_len, gate_w):

    def _A_ref(x, wqkv, wo):
        B, S, D = x.shape
        H = 32
        Dh = D // H
        W = 256
        qkv = (lambda x, *d: x.permute(*d))((x @ wqkv).reshape(B, S, 3, H, Dh), 2, 0, 3, 1, 4)
        q, k, v = (qkv[0], qkv[1], qkv[2])
        scores = torch.einsum('bhqd,bhkd->bhqk', q, k) * Dh ** (-0.5)
        idx = torch.arange(S)
        mask = (idx[:, None] - idx[None, :] >= 0) & (idx[:, None] - idx[None, :] < W)
        scores = torch.where(mask[None, None], scores, -1000000000.0)
        p = _softmax(scores, axis=-1)
        o = torch.einsum('bhqk,bhkd->bhqd', p, v)
        return (lambda x, *d: x.permute(*d))(o, 0, 2, 1, 3).reshape(B, S, D) @ wo

    def _B_ref(x, mem0, Wq, Wk, Wv, Wo, seg_len):
        B, S, Dm = x.shape
        H, Dh = (Wq.shape[1], Wq.shape[2])
        M = mem0.shape[1]
        scale = Dh ** (-0.5)
        n_seg = S // seg_len
        L = M + seg_len
        idx = torch.arange(L)
        mask = (idx[:, None] < M) | (idx[:, None] >= idx[None, :])
        mem = mem0
        outs = []
        for s in range(n_seg):
            xs = x[:, s * seg_len:(s + 1) * seg_len, :].clone()
            h = torch.concatenate([mem, xs], axis=1)
            q = torch.einsum('btd,dhe->bhte', h, Wq)
            k = torch.einsum('btd,dhe->bhte', h, Wk)
            v = torch.einsum('btd,dhe->bhte', h, Wv)
            scores = torch.einsum('bhqe,bhke->bhqk', q, k) * scale
            scores = torch.where(mask[None, None], scores, -1000000000.0)
            p = _softmax(scores, axis=-1)
            o = torch.einsum('bhqk,bhke->bhqe', p, v)
            o = torch.einsum('bhte,hed->btd', o, Wo)
            mem = o[:, :M, :].clone()
            outs.append(o[:, M:, :])
        return torch.concatenate(outs, axis=1)
    x = A_x
    g = _sigmoid((x * gate_w).sum(-1, keepdims=True))
    a = _A_ref(x, A_wqkv, A_wo)
    b = _B_ref(x, B_mem0, B_Wq, B_Wk, B_Wv, B_Wo, B_seg_len)
    return g * a + (1 - g) * b
```

```cpp
#include <hip/hip_runtime.h>
#include <hip/hip_bf16.h>
namespace naive {
typedef unsigned short u16;
__device__ __forceinline__ float b2f(u16 x) { return __uint_as_float((unsigned)x << 16); }
__device__ __forceinline__ u16 f2b(float f) { __bf16 b = (__bf16)f; return __builtin_bit_cast(u16, b); }
__device__ __forceinline__ float rb(float f) { return b2f(f2b(f)); }
__global__ void gemm(const u16* A, int lda, const u16* B, int ldb, u16* C, int ldc, int M, int N, int K) {
    __shared__ float As[16][17], Bs[16][17];
    int tx = threadIdx.x & 15, ty = threadIdx.x >> 4;
    int row = blockIdx.y * 16 + ty, col = blockIdx.x * 16 + tx;
    float acc = 0.f;
    for (int k0 = 0; k0 < K; k0 += 16) {
        As[ty][tx] = row < M ? b2f(A[(size_t)row * lda + k0 + tx]) : 0.f;
        Bs[ty][tx] = b2f(B[(size_t)(k0 + ty) * ldb + col]);
        __syncthreads();
        for (int k = 0; k < 16; ++k) acc += As[ty][k] * Bs[k][tx];
        __syncthreads();
    }
    if (row < M) C[(size_t)row * ldc + col] = f2b(acc);
}
__global__ void attnA(const u16* qkv, u16* o) {
    int idx = blockIdx.x * blockDim.x + threadIdx.x; int h = idx & 31, t = idx >> 5;
    float q[16]; for (int i = 0; i < 16; ++i) q[i] = b2f(qkv[(size_t)t * 1536 + h * 16 + i]);
    int k0 = t - 255 < 0 ? 0 : t - 255;
    float m = -1e30f;
    for (int k = k0; k <= t; ++k) { float s = 0; for (int i = 0; i < 16; ++i) s += q[i] * b2f(qkv[(size_t)k * 1536 + 512 + h * 16 + i]); s = rb(rb(s) * 0.25f); m = fmaxf(m, s); }
    float l = 0;
    for (int k = k0; k <= t; ++k) { float s = 0; for (int i = 0; i < 16; ++i) s += q[i] * b2f(qkv[(size_t)k * 1536 + 512 + h * 16 + i]); s = rb(rb(s) * 0.25f); l += expf(s - m); }
    float acc[16]; for (int i = 0; i < 16; ++i) acc[i] = 0;
    for (int k = k0; k <= t; ++k) { float s = 0; for (int i = 0; i < 16; ++i) s += q[i] * b2f(qkv[(size_t)k * 1536 + 512 + h * 16 + i]); s = rb(rb(s) * 0.25f); float p = rb(expf(s - m) / l);
        for (int i = 0; i < 16; ++i) acc[i] += p * b2f(qkv[(size_t)k * 1536 + 1024 + h * 16 + i]); }
    for (int i = 0; i < 16; ++i) o[(size_t)t * 512 + h * 16 + i] = f2b(acc[i]);
}
__global__ void attnB(const u16* q, const u16* k, const u16* v, u16* o) {
    __shared__ float p[40][41];
    int h = blockIdx.x; int tid = threadIdx.x;
    const float scale = rb(0.08838834764831845f);
    for (int i = tid; i < 1600; i += blockDim.x) { int a = i / 40, b = i % 40; float s = 0; for (int e = 0; e < 128; ++e) s += b2f(q[a * 4096 + h * 128 + e]) * b2f(k[b * 4096 + h * 128 + e]);
        s = rb(rb(s) * 0.08838834764831845f); if (!(a < 8 || a >= b)) s = -1e9f; p[a][b] = s; }
    __syncthreads();
    if (tid < 40) { float m = -1e30f; for (int b = 0; b < 40; ++b) m = fmaxf(m, p[tid][b]); float l = 0; for (int b = 0; b < 40; ++b) l += expf(p[tid][b] - m); for (int b = 0; b < 40; ++b) p[tid][b] = rb(expf(p[tid][b] - m) / l); }
    __syncthreads();
    for (int i = tid; i < 40 * 128; i += blockDim.x) { int a = i / 128, e = i % 128; float s = 0; for (int b = 0; b < 40; ++b) s += p[a][b] * b2f(v[b * 4096 + h * 128 + e]); o[a * 4096 + h * 128 + e] = f2b(s); }
    (void)scale;
}
__global__ void mix(const u16* x, const u16* gw, const u16* a, const u16* b, u16* out) {
    int t = blockIdx.x; __shared__ float red[256];
    float s = 0; for (int i = threadIdx.x; i < 512; i += 256) s += rb(b2f(x[t * 512 + i]) * b2f(gw[i]));
    red[threadIdx.x] = s; __syncthreads();
    for (int o = 128; o > 0; o >>= 1) { if (threadIdx.x < o) red[threadIdx.x] += red[threadIdx.x + o]; __syncthreads(); }
    float z = rb(red[0]); float g = rb(1.f / (1.f + expf(-z))); float og = rb(1.f - g);
    for (int i = threadIdx.x; i < 512; i += 256) out[t * 512 + i] = f2b(rb(g * b2f(a[t * 512 + i])) + rb(og * b2f(b[t * 512 + i])));
}
inline void run(void* const* d_in, void* d_out, void* d_ws, hipStream_t st, u16* mem_hist = nullptr) {
    const u16* x = (const u16*)d_in[0]; const u16* wqkv = (const u16*)d_in[1]; const u16* wo = (const u16*)d_in[2];
    const u16* mem0 = (const u16*)d_in[4]; const u16* Wq = (const u16*)d_in[5]; const u16* Wk = (const u16*)d_in[6]; const u16* Wv = (const u16*)d_in[7]; const u16* Wo = (const u16*)d_in[8]; const u16* gw = (const u16*)d_in[10];
    u16* ws = (u16*)d_ws;
    u16* qkv = ws; u16* oa = qkv + 2048 * 1536; u16* a = oa + 2048 * 512; u16* b = a + 2048 * 512; u16* hb = b + 2048 * 512; u16* q = hb + 48 * 512; u16* k = q + 48 * 4096; u16* v = k + 48 * 4096; u16* o = v + 48 * 4096; u16* ob = o + 48 * 4096;
    gemm<<<dim3(1536 / 16, 128), 256, 0, st>>>(x, 512, wqkv, 1536, qkv, 1536, 2048, 1536, 512);
    attnA<<<2048 * 32 / 256, 256, 0, st>>>(qkv, oa);
    gemm<<<dim3(32, 128), 256, 0, st>>>(oa, 512, wo, 512, a, 512, 2048, 512, 512);
    hipMemcpyAsync(hb, mem0, 8 * 512 * 2, hipMemcpyDeviceToDevice, st);
    for (int s = 0; s < 64; ++s) {
        if (mem_hist) hipMemcpyAsync(mem_hist + s * 4096, hb, 8192, hipMemcpyDeviceToDevice, st);
        hipMemcpyAsync(hb + 8 * 512, x + s * 32 * 512, 32 * 512 * 2, hipMemcpyDeviceToDevice, st);
        gemm<<<dim3(256, 3), 256, 0, st>>>(hb, 512, Wq, 4096, q, 4096, 40, 4096, 512);
        gemm<<<dim3(256, 3), 256, 0, st>>>(hb, 512, Wk, 4096, k, 4096, 40, 4096, 512);
        gemm<<<dim3(256, 3), 256, 0, st>>>(hb, 512, Wv, 4096, v, 4096, 40, 4096, 512);
        attnB<<<32, 256, 0, st>>>(q, k, v, o);
        gemm<<<dim3(32, 3), 256, 0, st>>>(o, 4096, Wo, 512, ob, 512, 40, 512, 4096);
        hipMemcpyAsync(hb, ob, 8 * 512 * 2, hipMemcpyDeviceToDevice, st);
        hipMemcpyAsync(b + s * 32 * 512, ob + 8 * 512, 32 * 512 * 2, hipMemcpyDeviceToDevice, st);
    }
    mix<<<2048, 256, 0, st>>>(x, gw, a, b, (u16*)d_out);
}
}
extern "C" void kernel_launch(void* const* d_in, const int* in_sizes, int n_in, void* d_out, int out_size, void* d_ws, size_t ws_size, hipStream_t stream) {
    naive::run(d_in, d_out, d_ws, stream);
}
```

```cpp
#include <hip/hip_runtime.h>
#include <stdint.h>

typedef unsigned short u16;
typedef unsigned u32;
typedef u32 u32x2 __attribute__((ext_vector_type(2)));
typedef u32 u32x4 __attribute__((ext_vector_type(4)));
typedef float f32x2 __attribute__((ext_vector_type(2)));
typedef float f32x4 __attribute__((ext_vector_type(4)));
typedef short s16x4 __attribute__((ext_vector_type(4)));
typedef __bf16 bf16x2 __attribute__((ext_vector_type(2)));
typedef __bf16 bf16x8 __attribute__((ext_vector_type(8)));
#define LAS __attribute__((address_space(3)))
#define GAS __attribute__((address_space(1)))
typedef GAS u32 gu32;
typedef GAS unsigned long long gu64;
typedef __amdgpu_buffer_rsrc_t rsrc_t;
#define RLX_AGENT __ATOMIC_RELAXED, __HIP_MEMORY_SCOPE_AGENT

constexpr u32 MB = 1u << 20;
constexpr u32 OFF_COMM = 0;
constexpr u32 L_PART = 0;
constexpr u32 L_MEM = 512 * 1024;
constexpr u32 OFF_CTRL = 2 * MB;
constexpr u32 C_ABORT = 0, C_CALL = 256, C_INIT = 1024, C_PROG = 4096, C_DONEA = 40960, C_KM = 262144, C_DONEB = 2 * MB;
constexpr u32 OFF_GATE = 2 * MB + 131072;
constexpr u32 OFF_KM = 6 * MB, OFF_KF = 16 * MB, OFF_VF = 32 * MB, OFF_QF = 48 * MB, OFF_OF = 64 * MB;
constexpr u32 OFF_QA = 84 * MB, OFF_KA = 86 * MB, OFF_VA = 88 * MB, OFF_OA = 90 * MB, WS_USED = 92 * MB;
constexpr u32 SENT = 0xFF81FF81u;
constexpr int LDS_BYTES = 163840;
constexpr u32 SPIN_CAP = 400000;

constexpr int PSLEEP = 2;
constexpr int ASPLIT = 1280;
constexpr int QSEG = 56;
constexpr int WCH = 32;
struct Params {
    const u16 *x, *wqkv, *wo, *mem0, *Wq, *Wk, *Wv, *Wo, *gw;
    u16* out;
    char* ws;
};

__device__ __forceinline__ float b2f(u32 h) { return __uint_as_float(h << 16); }
__device__ __forceinline__ u32 pk2(float a, float b) { f32x2 f = {a, b}; return __builtin_bit_cast(u32, __builtin_convertvector(f, bf16x2)); }
__device__ __forceinline__ float rb(float a) { return __uint_as_float(pk2(a, 0.f) << 16); }
__device__ __forceinline__ u32x2 pk4(f32x4 a) { u32x2 r = {pk2(a[0], a[1]), pk2(a[2], a[3])}; return r; }
__device__ __forceinline__ u32x4 pk8(f32x4 a, f32x4 b) { u32x4 r = {pk2(a[0], a[1]), pk2(a[2], a[3]), pk2(b[0], b[1]), pk2(b[2], b[3])}; return r; }
__device__ __forceinline__ f32x4 mfma32(u32x4 a, u32x4 b, f32x4 c) { return __builtin_amdgcn_mfma_f32_16x16x32_bf16(__builtin_bit_cast(bf16x8, a), __builtin_bit_cast(bf16x8, b), c, 0, 0, 0); }
__device__ __forceinline__ f32x4 mfma16(u32x2 a, u32x2 b, f32x4 c) { return __builtin_amdgcn_mfma_f32_16x16x16bf16_1k(__builtin_bit_cast(s16x4, a), __builtin_bit_cast(s16x4, b), c, 0, 0, 0); }
__device__ __forceinline__ u32x2 trd(LAS char* p) { return __builtin_bit_cast(u32x2, __builtin_amdgcn_ds_read_tr16_b64_v4i16((LAS s16x4*)p)); }
__device__ __forceinline__ u32x4 cat(u32x2 a, u32x2 b) { u32x4 r = {a[0], a[1], b[0], b[1]}; return r; }
__device__ __forceinline__ u32x4 ld16(rsrc_t r, u32 off) { return __builtin_amdgcn_raw_buffer_load_b128(r, off, 0, 16); }
__device__ __forceinline__ u32x4 ld16s(rsrc_t r, u32 off, u32 soff) { return __builtin_amdgcn_raw_buffer_load_b128(r, off, soff, 16); }
__device__ __forceinline__ void st16(rsrc_t r, u32 off, u32x4 v) { __builtin_amdgcn_raw_buffer_store_b128(v, r, off, 0, 16); }
__device__ __forceinline__ void st8(rsrc_t r, u32 off, u32x2 v) { __builtin_amdgcn_raw_buffer_store_b64(v, r, off, 0, 16); }
__device__ __forceinline__ void st4(rsrc_t r, u32 off, u32 v) { __builtin_amdgcn_raw_buffer_store_b32(v, r, off, 0, 16); }
__device__ __forceinline__ void drain() { asm volatile("s_waitcnt vmcnt(0)" ::: "memory"); }
__device__ __forceinline__ void bar() { asm volatile("s_waitcnt lgkmcnt(0)\n\ts_barrier" ::: "memory"); }
__device__ __forceinline__ void fence_c() { asm volatile("" ::: "memory"); }
#define SB() __builtin_amdgcn_sched_barrier(0)
__device__ __forceinline__ float fx(u32 a) { return __uint_as_float(a); }
__device__ __forceinline__ u32 xf(float a) { return __float_as_uint(a); }
__device__ __forceinline__ float allmax_g(float x) {
    auto r = __builtin_amdgcn_permlane16_swap(xf(x), xf(x), false, false); x = fmaxf(fx(r[0]), fx(r[1]));
    auto q = __builtin_amdgcn_permlane32_swap(xf(x), xf(x), false, false); return fmaxf(fx(q[0]), fx(q[1])); }
__device__ __forceinline__ float allsum_g(float x) {
    auto r = __builtin_amdgcn_permlane16_swap(xf(x), xf(x), false, false); x = fx(r[0]) + fx(r[1]);
    auto q = __builtin_amdgcn_permlane32_swap(xf(x), xf(x), false, false); return fx(q[0]) + fx(q[1]); }
__device__ __forceinline__ u32 comm_off(u32 L, u32 pol) { return OFF_COMM + (((L >> 13) << 14) | (pol << 13) | (L & 8191u)); }
struct Ctl { char* base; u32 tag; };
__device__ __forceinline__ void put_flag(const Ctl& k, u32 off, u32 v) { __hip_atomic_store((gu64*)(k.base + off), ((unsigned long long)k.tag << 32) | v, RLX_AGENT); }
__device__ __forceinline__ bool spin_fail(u32& spins, const Ctl& k) {
    ++spins;
    if ((spins & 63u) != 0u) return false;
    if (spins > SPIN_CAP) { u32 t = k.tag; asm volatile("" : "+s"(t)); __hip_atomic_store((gu32*)(k.base + C_ABORT + 4), t, RLX_AGENT); return true; }
    return (u32)__builtin_amdgcn_readfirstlane(__hip_atomic_load((gu32*)(k.base + C_ABORT + 4), RLX_AGENT)) == k.tag;
}
template <int SLEEP = PSLEEP>
__device__ __forceinline__ void wait_flags(const Ctl& k, u32 off, u32 want) {
    u32 spins = 0;
    for (;;) {
        const unsigned long long v = __hip_atomic_load((gu64*)(k.base + off), RLX_AGENT);
        if (__all((u32)(v >> 32) == k.tag && (u32)v - want <= 64u)) break;
        if (spin_fail(spins, k)) break;
        __builtin_amdgcn_s_sleep(SLEEP);
    }
    fence_c();
}

constexpr u32 CODE_LEN = 119808;
template <bool AHEAD>
__device__ __forceinline__ u32 code_touch(int piece, int lane) {
    unsigned long long pc; u32 back;
    asm volatile("s_getpc_b64 %0\nLCT_%=:\n\ts_mov_b32 %1, LCT_%=-_Z11main_kernel6Params" : "=s"(pc), "=s"(back));
    return *(const GAS u32*)(pc - back + min((AHEAD ? back : 0u) + (u32)(piece * 4096 + lane * 64), CODE_LEN - 4u));
}

__device__ __forceinline__ void setup_helper(const Params& p, const Ctl& k, LAS char* lds, rsrc_t rs, int j, int lane, int wave) {
    u32 half = 0;
    if (j == 0) {
        u32 best[2] = {~0u, ~0u};
#pragma unroll 1
        for (int i = 0; i < 12; ++i) {
            unsigned long long t0, t1;
            asm volatile("s_memtime %0\n\ts_waitcnt lgkmcnt(0)" : "=s"(t0) :: "memory");
            if (lane < 32) st4(rs, OFF_COMM + (u32)(wave * 16384 + (i & 1) * 8192 + wave * 2048 + lane * 4), SENT);
            drain();
            asm volatile("s_memtime %0\n\ts_waitcnt lgkmcnt(0)" : "=s"(t1) :: "memory");
            const u32 d = (u32)(t1 - t0);
            if (i >= 2) { if (i & 1) best[1] = min(best[1], d); else best[0] = min(best[0], d); }
        }
        if (lane == 0) { ((LAS u32*)lds)[wave * 2] = best[0]; ((LAS u32*)lds)[wave * 2 + 1] = best[1]; }
        bar();
        const u32x4 a = *(LAS u32x4*)lds, b = *(LAS u32x4*)(lds + 16);
        half = (a[0] + a[2]) + (b[0] + b[2]) <= (a[1] + a[3]) + (b[1] + b[3]) ? 1u : 0u;
    }
    const u32x4 e = {SENT, SENT, SENT, SENT};
    const int n0 = j == 0 ? 0 : 8 + (j - 1) * 72, n1 = 8 + j * 72;
#pragma unroll 8
    for (int n = n0; n < n1; ++n) st16(rs, OFF_COMM + (u32)(n * 4096 + wave * 1024 + lane * 16), e);
    const u32x4 nt = {~0u, k.tag - 1u, ~0u, k.tag - 1u};
    for (int n = j; n < 65; n += 8) st16(rs, OFF_CTRL + C_KM + (u32)(n * 4096 + wave * 1024 + lane * 16), nt);
    drain();
    bar();
    if (wave == 0 && lane < 16) put_flag(k, C_INIT + j * 128 + lane * 8, 2u + half);
    for (int n = j; n < 132; n += 8) st16(rs, OFF_CTRL + (n < 4 ? C_DONEA + (u32)n * 4096u : C_DONEB + (u32)(n - 4) * 4096u) + (u32)(wave * 1024 + lane * 16), nt);
    const u16* gp = p.gw + (lane & 15) * 8;
    u32x4 gw[4];
#pragma unroll
    for (int i = 0; i < 4; ++i) gw[i] = *(const u32x4*)(gp + i * 128);
    float zsum = 0.f;
#pragma unroll 4
    for (int r = 0; r < 16; ++r) {
        const int t = j * 256 + wave * 64 + (lane >> 4) * 16 + r;
        float s = 0.f;
#pragma unroll
        for (int i = 0; i < 4; ++i) {
            const u32x4 xv = *(const u32x4*)(p.x + (size_t)t * 512 + (lane & 15) * 8 + i * 128);
#pragma unroll
            for (int q = 0; q < 4; ++q) { s += rb(b2f(xv[q] & 0xffffu) * b2f(gw[i][q] & 0xffffu)); s += rb(b2f(xv[q] >> 16) * b2f(gw[i][q] >> 16)); }
        }
#pragma unroll
        for (int o = 8; o > 0; o >>= 1) s += __shfl_xor(s, o);
        if ((lane & 15) == r) zsum = s;
    }
    st4(rs, OFF_GATE + (u32)(j * 256 + wave * 64 + lane) * 4u, xf(rb(1.f / (1.f + __expf(-rb(zsum))))));
    drain();
    bar();
    if (wave == 0 && lane < 16) put_flag(k, C_INIT + 1024 + j * 128 + lane * 8, 1u);
}

template <int ORD, int NI>
__device__ __forceinline__ void stage32_load(const u16* src, int ld, int lane, u32x4 (&st)[NI], int i0) {
    const u16* s = src + (size_t)(lane >> 2) * ld + (lane & 3) * 8;
#pragma unroll
    for (int i = 0; i < NI; ++i) st[i] = *(const u32x4*)(s + (size_t)(i0 + i) * 16 * ld);
}
template <int ORD, int NI>
__device__ __forceinline__ void stage32_write(LAS char* scr, int lane, const u32x4 (&st)[NI]) {
    const int t = (lane >> 1) & 1, q = ((lane >> 2) & 3) ^ (2 * t);
    const int base = ORD == 0 ? (t * 1024 + ((lane >> 4) & 1) * 512 + (lane >> 5) * 128 + q * 32 + (lane & 1) * 16)
                              : (t * 1024 + (lane >> 4) * 128 + q * 32 + (lane & 1) * 16);
#pragma unroll
    for (int i = 0; i < NI; ++i) *(LAS u32x4*)(scr + base + (i >> 1) * 2048 + (i & 1) * (ORD == 0 ? 256 : 512)) = st[i];
}
__device__ __forceinline__ u32x4 stage32_frag(LAS char* scr, int lane, int kk, int t) {
    LAS char* a = scr + ((lane * 8) ^ (t * 64)) + kk * 2048 + t * 1024;
    return cat(trd(a), trd(a + 512));
}

#define CHAIN_ASM \
    "s_waitcnt vmcnt(0) lgkmcnt(0)\n\t" \
    "s_getpc_b64 s[82:83]\n\t" \
    "LPF_%=:\n\t" \
    "s_lshl_b32 s75, %[wv], 12\n\t" \
    "v_lshlrev_b32 v113, 2, %[l16]\n\t" \
    "v_add_u32 v113, s75, v113\n\t" \
    "v_min_u32 v113, LEND_%=-LPF_%=-4, v113\n\t" \
    "global_load_dword v115, v113, s[82:83]\n\t" \
    "ds_read_b128 v[14:17], %[mw] offset:8192\n\t" \
    "ds_read_b64 v[18:19], %[mw] offset:9216\n\t" \
    "ds_read_b32 v114, %[mw] offset:9224\n\t" \
    "s_mov_b32 s78, 0x3e0293ee\n\t" \
    "s_mov_b32 s79, 0x3e0293ee\n\t" \
    "s_mov_b32 s76, 0x200000\n\t" \
    "s_lshr_b32 s75, %[smld], 11\n\t" \
    "s_and_b32 s75, s75, 3\n\t" \
    "s_xor_b32 s61, s75, 0\n\t" \
    "s_lshl_b32 s61, s61, 20\n\t" \
    "s_xor_b32 s62, s75, 1\n\t" \
    "s_lshl_b32 s62, s62, 20\n\t" \
    "s_xor_b32 s63, s75, 2\n\t" \
    "s_lshl_b32 s63, s63, 20\n\t" \
    "s_xor_b32 s64, s75, 3\n\t" \
    "s_lshl_b32 s64, s64, 20\n\t" \
    "buffer_load_dword v124, off, %[rs], s76 offset:256 sc1\n\t" \
    "s_waitcnt lgkmcnt(1)\n\t" \
    "buffer_load_dword v116, v19, %[rm], 0 offen\n\t" \
    "buffer_load_dword v117, v19, %[rm], 0 offen offset:8\n\t" \
    "buffer_load_dword v118, v19, %[rm], 0 offen offset:16\n\t" \
    "buffer_load_dword v119, v19, %[rm], 0 offen offset:24\n\t" \
    "buffer_load_dword v120, v19, %[rm], 0 offen offset:128\n\t" \
    "buffer_load_dword v121, v19, %[rm], 0 offen offset:136\n\t" \
    "buffer_load_dword v122, v19, %[rm], 0 offen offset:144\n\t" \
    "buffer_load_dword v123, v19, %[rm], 0 offen offset:152\n\t" \
    "s_add_u32 s75, s61, 0\n\t" \
    "buffer_load_dwordx4 v[128:131], v14, %[rq], s75 offen\n\t" \
    "s_add_u32 s75, s61, 131072\n\t" \
    "buffer_load_dwordx4 v[132:135], v14, %[rq], s75 offen\n\t" \
    "s_add_u32 s75, s61, 262144\n\t" \
    "buffer_load_dwordx4 v[144:147], v14, %[rq], s75 offen\n\t" \
    "s_add_u32 s75, s61, 393216\n\t" \
    "buffer_load_dwordx4 v[148:151], v14, %[rq], s75 offen\n\t" \
    "s_add_u32 s75, s62, 0\n\t" \
    "buffer_load_dwordx4 v[20:23], v14, %[rq], s75 offen\n\t" \
    "s_add_u32 s75, s62, 131072\n\t" \
    "buffer_load_dwordx4 v[24:27], v14, %[rq], s75 offen\n\t" \
    "s_add_u32 s75, s62, 262144\n\t" \
    "buffer_load_dwordx4 v[28:31], v14, %[rq], s75 offen\n\t" \
    "s_add_u32 s75, s62, 393216\n\t" \
    "buffer_load_dwordx4 v[32:35], v14, %[rq], s75 offen\n\t" \
    "s_add_u32 s75, s61, 0\n\t" \
    "buffer_load_dwordx4 v[192:195], v14, %[rk], s75 offen\n\t" \
    "s_add_u32 s75, s61, 131072\n\t" \
    "buffer_load_dwordx4 v[196:199], v14, %[rk], s75 offen\n\t" \
    "s_add_u32 s75, s61, 262144\n\t" \
    "buffer_load_dwordx4 v[208:211], v14, %[rk], s75 offen\n\t" \
    "s_add_u32 s75, s61, 393216\n\t" \
    "buffer_load_dwordx4 v[212:215], v14, %[rk], s75 offen\n\t" \
    "s_add_u32 s75, s62, 0\n\t" \
    "buffer_load_dwordx4 a[192:195], v14, %[rk], s75 offen\n\t" \
    "s_add_u32 s75, s62, 131072\n\t" \
    "buffer_load_dwordx4 a[196:199], v14, %[rk], s75 offen\n\t" \
    "s_add_u32 s75, s62, 262144\n\t" \
    "buffer_load_dwordx4 a[208:211], v14, %[rk], s75 offen\n\t" \
    "s_add_u32 s75, s62, 393216\n\t" \
    "buffer_load_dwordx4 a[212:215], v14, %[rk], s75 offen\n\t" \
    "s_add_u32 s75, s61, 0\n\t" \
    "buffer_load_dwordx4 a[0:3], v14, %[rv], s75 offen\n\t" \
    "s_add_u32 s75, s61, 131072\n\t" \
    "buffer_load_dwordx4 a[4:7], v14, %[rv], s75 offen\n\t" \
    "s_add_u32 s75, s61, 262144\n\t" \
    "buffer_load_dwordx4 a[8:11], v14, %[rv], s75 offen\n\t" \
    "s_add_u32 s75, s61, 393216\n\t" \
    "buffer_load_dwordx4 a[12:15], v14, %[rv], s75 offen\n\t" \
    "s_add_u32 s75, s62, 0\n\t" \
    "buffer_load_dwordx4 a[32:35], v14, %[rv], s75 offen\n\t" \
    "s_add_u32 s75, s62, 131072\n\t" \
    "buffer_load_dwordx4 a[36:39], v14, %[rv], s75 offen\n\t" \
    "s_add_u32 s75, s62, 262144\n\t" \
    "buffer_load_dwordx4 a[40:43], v14, %[rv], s75 offen\n\t" \
    "s_add_u32 s75, s62, 393216\n\t" \
    "buffer_load_dwordx4 a[44:47], v14, %[rv], s75 offen\n\t" \
    "s_mov_b32 s75, 0\n\t" \
    "buffer_load_dwordx4 a[128:131], v16, %[ro], s75 offen\n\t" \
    "s_mov_b32 s75, 8192\n\t" \
    "buffer_load_dwordx4 a[132:135], v16, %[ro], s75 offen\n\t" \
    "s_mov_b32 s75, 16384\n\t" \
    "buffer_load_dwordx4 a[136:139], v16, %[ro], s75 offen\n\t" \
    "s_mov_b32 s75, 24576\n\t" \
    "buffer_load_dwordx4 a[140:143], v16, %[ro], s75 offen\n\t" \
    "s_mov_b32 s75, 32768\n\t" \
    "buffer_load_dwordx4 a[144:147], v16, %[ro], s75 offen\n\t" \
    "s_mov_b32 s75, 40960\n\t" \
    "buffer_load_dwordx4 a[148:151], v16, %[ro], s75 offen\n\t" \
    "s_mov_b32 s75, 49152\n\t" \
    "buffer_load_dwordx4 a[152:155], v16, %[ro], s75 offen\n\t" \
    "s_mov_b32 s75, 57344\n\t" \
    "buffer_load_dwordx4 a[156:159], v16, %[ro], s75 offen\n\t" \
    "v_xor_b32 v125, 64, v17\n\t" \
    "v_xor_b32 v126, 64, v18\n\t" \
    "s_waitcnt vmcnt(31)\n\t" \
    "ds_write_b128 v15, v[128:131] offset:16384\n\t" \
    "s_waitcnt vmcnt(30)\n\t" \
    "ds_write_b128 v15, v[132:135] offset:16640\n\t" \
    "s_waitcnt vmcnt(29)\n\t" \
    "ds_write_b128 v15, v[144:147] offset:18432\n\t" \
    "s_waitcnt vmcnt(28)\n\t" \
    "ds_write_b128 v15, v[148:151] offset:18688\n\t" \
    "s_waitcnt vmcnt(27)\n\t" \
    "ds_write_b128 v15, v[20:23] offset:20480\n\t" \
    "s_waitcnt vmcnt(26)\n\t" \
    "ds_write_b128 v15, v[24:27] offset:20736\n\t" \
    "s_waitcnt vmcnt(25)\n\t" \
    "ds_write_b128 v15, v[28:31] offset:22528\n\t" \
    "s_waitcnt vmcnt(24)\n\t" \
    "ds_write_b128 v15, v[32:35] offset:22784\n\t" \
    "s_add_u32 s75, s63, 0\n\t" \
    "buffer_load_dwordx4 v[160:163], v14, %[rq], s75 offen\n\t" \
    "s_add_u32 s75, s63, 131072\n\t" \
    "buffer_load_dwordx4 v[164:167], v14, %[rq], s75 offen\n\t" \
    "s_add_u32 s75, s63, 262144\n\t" \
    "buffer_load_dwordx4 v[176:179], v14, %[rq], s75 offen\n\t" \
    "s_add_u32 s75, s63, 393216\n\t" \
    "buffer_load_dwordx4 v[180:183], v14, %[rq], s75 offen\n\t" \
    "s_add_u32 s75, s64, 0\n\t" \
    "buffer_load_dwordx4 v[52:55], v14, %[rq], s75 offen\n\t" \
    "s_add_u32 s75, s64, 131072\n\t" \
    "buffer_load_dwordx4 v[56:59], v14, %[rq], s75 offen\n\t" \
    "s_add_u32 s75, s64, 262144\n\t" \
    "buffer_load_dwordx4 v[60:63], v14, %[rq], s75 offen\n\t" \
    "s_add_u32 s75, s64, 393216\n\t" \
    "buffer_load_dwordx4 v[64:67], v14, %[rq], s75 offen\n\t" \
    "ds_read_b64_tr_b16 v[128:129], v18 offset:16384\n\t" \
    "ds_read_b64_tr_b16 v[130:131], v18 offset:16896\n\t" \
    "ds_read_b64_tr_b16 v[132:133], v18 offset:18432\n\t" \
    "ds_read_b64_tr_b16 v[134:135], v18 offset:18944\n\t" \
    "ds_read_b64_tr_b16 v[20:21], v126 offset:17408\n\t" \
    "ds_read_b64_tr_b16 v[22:23], v126 offset:17920\n\t" \
    "s_waitcnt lgkmcnt(7)\n\t" \
    "ds_read_b64_tr_b16 v[24:25], v126 offset:19456\n\t" \
    "ds_read_b64_tr_b16 v[26:27], v126 offset:19968\n\t" \
    "ds_read_b64_tr_b16 v[144:145], v18 offset:20480\n\t" \
    "ds_read_b64_tr_b16 v[146:147], v18 offset:20992\n\t" \
    "ds_read_b64_tr_b16 v[148:149], v18 offset:22528\n\t" \
    "ds_read_b64_tr_b16 v[150:151], v18 offset:23040\n\t" \
    "ds_read_b64_tr_b16 v[28:29], v126 offset:21504\n\t" \
    "ds_read_b64_tr_b16 v[30:31], v126 offset:22016\n\t" \
    "s_waitcnt lgkmcnt(7)\n\t" \
    "ds_read_b64_tr_b16 v[32:33], v126 offset:23552\n\t" \
    "ds_read_b64_tr_b16 v[34:35], v126 offset:24064\n\t" \
    "s_waitcnt vmcnt(31)\n\t" \
    "ds_write_b128 v15, v[192:195] offset:24576\n\t" \
    "s_waitcnt vmcnt(30)\n\t" \
    "ds_write_b128 v15, v[196:199] offset:24832\n\t" \
    "s_waitcnt vmcnt(29)\n\t" \
    "ds_write_b128 v15, v[208:211] offset:26624\n\t" \
    "s_waitcnt vmcnt(28)\n\t" \
    "ds_write_b128 v15, v[212:215] offset:26880\n\t" \
    "s_waitcnt vmcnt(27)\n\t" \
    "ds_write_b128 v15, a[192:195] offset:28672\n\t" \
    "s_waitcnt vmcnt(26)\n\t" \
    "ds_write_b128 v15, a[196:199] offset:28928\n\t" \
    "s_waitcnt vmcnt(25)\n\t" \
    "s_waitcnt lgkmcnt(7)\n\t" \
    "ds_write_b128 v15, a[208:211] offset:30720\n\t" \
    "s_waitcnt vmcnt(24)\n\t" \
    "ds_write_b128 v15, a[212:215] offset:30976\n\t" \
    "ds_write_b128 %[wob], v[20:23]\n\t" \
    "ds_write_b128 %[wob], v[24:27] offset:1024\n\t" \
    "ds_write_b128 %[wob], v[28:31] offset:4096\n\t" \
    "s_waitcnt lgkmcnt(11)\n\t" \
    "ds_write_b128 %[wob], v[32:35] offset:5120\n\t" \
    "s_add_u32 s75, s63, 0\n\t" \
    "buffer_load_dwordx4 v[224:227], v14, %[rk], s75 offen\n\t" \
    "s_add_u32 s75, s63, 131072\n\t" \
    "buffer_load_dwordx4 v[228:231], v14, %[rk], s75 offen\n\t" \
    "s_add_u32 s75, s63, 262144\n\t" \
    "buffer_load_dwordx4 v[240:243], v14, %[rk], s75 offen\n\t" \
    "s_add_u32 s75, s63, 393216\n\t" \
    "buffer_load_dwordx4 v[244:247], v14, %[rk], s75 offen\n\t" \
    "s_add_u32 s75, s64, 0\n\t" \
    "buffer_load_dwordx4 a[224:227], v14, %[rk], s75 offen\n\t" \
    "s_add_u32 s75, s64, 131072\n\t" \
    "buffer_load_dwordx4 a[228:231], v14, %[rk], s75 offen\n\t" \
    "s_add_u32 s75, s64, 262144\n\t" \
    "buffer_load_dwordx4 a[240:243], v14, %[rk], s75 offen\n\t" \
    "s_add_u32 s75, s64, 393216\n\t" \
    "buffer_load_dwordx4 a[244:247], v14, %[rk], s75 offen\n\t" \
    "ds_read_b64_tr_b16 v[192:193], v18 offset:24576\n\t" \
    "ds_read_b64_tr_b16 v[194:195], v18 offset:25088\n\t" \
    "ds_read_b64_tr_b16 v[196:197], v18 offset:26624\n\t" \
    "s_waitcnt lgkmcnt(7)\n\t" \
    "ds_read_b64_tr_b16 v[198:199], v18 offset:27136\n\t" \
    "ds_read_b64_tr_b16 a[192:193], v126 offset:25600\n\t" \
    "ds_read_b64_tr_b16 a[194:195], v126 offset:26112\n\t" \
    "ds_read_b64_tr_b16 a[196:197], v126 offset:27648\n\t" \
    "ds_read_b64_tr_b16 a[198:199], v126 offset:28160\n\t" \
    "ds_read_b64_tr_b16 v[208:209], v18 offset:28672\n\t" \
    "ds_read_b64_tr_b16 v[210:211], v18 offset:29184\n\t" \
    "ds_read_b64_tr_b16 v[212:213], v18 offset:30720\n\t" \
    "s_waitcnt lgkmcnt(7)\n\t" \
    "ds_read_b64_tr_b16 v[214:215], v18 offset:31232\n\t" \
    "ds_read_b64_tr_b16 a[208:209], v126 offset:29696\n\t" \
    "ds_read_b64_tr_b16 a[210:211], v126 offset:30208\n\t" \
    "ds_read_b64_tr_b16 a[212:213], v126 offset:31744\n\t" \
    "ds_read_b64_tr_b16 a[214:215], v126 offset:32256\n\t" \
    "s_waitcnt vmcnt(31)\n\t" \
    "ds_write_b128 v15, a[0:3] offset:16384\n\t" \
    "s_waitcnt vmcnt(30)\n\t" \
    "ds_write_b128 v15, a[4:7] offset:16640\n\t" \
    "s_waitcnt vmcnt(29)\n\t" \
    "ds_write_b128 v15, a[8:11] offset:18432\n\t" \
    "s_waitcnt vmcnt(28)\n\t" \
    "s_waitcnt lgkmcnt(7)\n\t" \
    "ds_write_b128 v15, a[12:15] offset:18688\n\t" \
    "s_waitcnt vmcnt(27)\n\t" \
    "ds_write_b128 v15, a[32:35] offset:20480\n\t" \
    "s_waitcnt vmcnt(26)\n\t" \
    "ds_write_b128 v15, a[36:39] offset:20736\n\t" \
    "s_waitcnt vmcnt(25)\n\t" \
    "ds_write_b128 v15, a[40:43] offset:22528\n\t" \
    "s_waitcnt vmcnt(24)\n\t" \
    "ds_write_b128 v15, a[44:47] offset:22784\n\t" \
    "s_add_u32 s75, s63, 0\n\t" \
    "buffer_load_dwordx4 a[64:67], v14, %[rv], s75 offen\n\t" \
    "s_add_u32 s75, s63, 131072\n\t" \
    "buffer_load_dwordx4 a[68:71], v14, %[rv], s75 offen\n\t" \
    "s_add_u32 s75, s63, 262144\n\t" \
    "buffer_load_dwordx4 a[72:75], v14, %[rv], s75 offen\n\t" \
    "s_add_u32 s75, s63, 393216\n\t" \
    "buffer_load_dwordx4 a[76:79], v14, %[rv], s75 offen\n\t" \
    "s_add_u32 s75, s64, 0\n\t" \
    "buffer_load_dwordx4 a[96:99], v14, %[rv], s75 offen\n\t" \
    "s_add_u32 s75, s64, 131072\n\t" \
    "buffer_load_dwordx4 a[100:103], v14, %[rv], s75 offen\n\t" \
    "s_add_u32 s75, s64, 262144\n\t" \
    "buffer_load_dwordx4 a[104:107], v14, %[rv], s75 offen\n\t" \
    "s_add_u32 s75, s64, 393216\n\t" \
    "buffer_load_dwordx4 a[108:111], v14, %[rv], s75 offen\n\t" \
    "ds_read_b64_tr_b16 a[0:1], v18 offset:16384\n\t" \
    "ds_read_b64_tr_b16 a[2:3], v18 offset:16896\n\t" \
    "ds_read_b64_tr_b16 a[4:5], v18 offset:18432\n\t" \
    "s_waitcnt lgkmcnt(7)\n\t" \
    "ds_read_b64_tr_b16 a[6:7], v18 offset:18944\n\t" \
    "ds_read_b64_tr_b16 a[8:9], v126 offset:17408\n\t" \
    "ds_read_b64_tr_b16 a[10:11], v126 offset:17920\n\t" \
    "ds_read_b64_tr_b16 a[12:13], v126 offset:19456\n\t" \
    "ds_read_b64_tr_b16 a[14:15], v126 offset:19968\n\t" \
    "ds_read_b64_tr_b16 a[32:33], v18 offset:20480\n\t" \
    "ds_read_b64_tr_b16 a[34:35], v18 offset:20992\n\t" \
    "ds_read_b64_tr_b16 a[36:37], v18 offset:22528\n\t" \
    "s_waitcnt lgkmcnt(7)\n\t" \
    "ds_read_b64_tr_b16 a[38:39], v18 offset:23040\n\t" \
    "ds_read_b64_tr_b16 a[40:41], v126 offset:21504\n\t" \
    "ds_read_b64_tr_b16 a[42:43], v126 offset:22016\n\t" \
    "ds_read_b64_tr_b16 a[44:45], v126 offset:23552\n\t" \
    "ds_read_b64_tr_b16 a[46:47], v126 offset:24064\n\t" \
    "s_waitcnt vmcnt(31)\n\t" \
    "ds_write_b64 v17, a[128:129] offset:24576\n\t" \
    "ds_write_b64 v17, a[130:131] offset:26624\n\t" \
    "s_waitcnt vmcnt(30)\n\t" \
    "ds_write_b64 v17, a[132:133] offset:25600\n\t" \
    "s_waitcnt lgkmcnt(7)\n\t" \
    "ds_write_b64 v17, a[134:135] offset:27648\n\t" \
    "s_waitcnt vmcnt(29)\n\t" \
    "ds_write_b64 v17, a[136:137] offset:24704\n\t" \
    "ds_write_b64 v17, a[138:139] offset:26752\n\t" \
    "s_waitcnt vmcnt(28)\n\t" \
    "ds_write_b64 v17, a[140:141] offset:25728\n\t" \
    "ds_write_b64 v17, a[142:143] offset:27776\n\t" \
    "s_waitcnt vmcnt(27)\n\t" \
    "ds_write_b64 v125, a[144:145] offset:24576\n\t" \
    "ds_write_b64 v125, a[146:147] offset:26624\n\t" \
    "s_waitcnt vmcnt(26)\n\t" \
    "ds_write_b64 v125, a[148:149] offset:25600\n\t" \
    "s_waitcnt lgkmcnt(7)\n\t" \
    "ds_write_b64 v125, a[150:151] offset:27648\n\t" \
    "s_waitcnt vmcnt(25)\n\t" \
    "ds_write_b64 v125, a[152:153] offset:24704\n\t" \
    "ds_write_b64 v125, a[154:155] offset:26752\n\t" \
    "s_waitcnt vmcnt(24)\n\t" \
    "ds_write_b64 v125, a[156:157] offset:25728\n\t" \
    "ds_write_b64 v125, a[158:159] offset:27776\n\t" \
    "s_add_u32 s75, s61, 524288\n\t" \
    "buffer_load_dwordx4 v[136:139], v14, %[rq], s75 offen\n\t" \
    "s_add_u32 s75, s61, 655360\n\t" \
    "buffer_load_dwordx4 v[140:143], v14, %[rq], s75 offen\n\t" \
    "s_add_u32 s75, s61, 786432\n\t" \
    "buffer_load_dwordx4 v[152:155], v14, %[rq], s75 offen\n\t" \
    "s_add_u32 s75, s61, 917504\n\t" \
    "buffer_load_dwordx4 v[156:159], v14, %[rq], s75 offen\n\t" \
    "s_add_u32 s75, s62, 524288\n\t" \
    "buffer_load_dwordx4 v[36:39], v14, %[rq], s75 offen\n\t" \
    "s_add_u32 s75, s62, 655360\n\t" \
    "buffer_load_dwordx4 v[40:43], v14, %[rq], s75 offen\n\t" \
    "s_add_u32 s75, s62, 786432\n\t" \
    "buffer_load_dwordx4 v[44:47], v14, %[rq], s75 offen\n\t" \
    "s_add_u32 s75, s62, 917504\n\t" \
    "buffer_load_dwordx4 v[48:51], v14, %[rq], s75 offen\n\t" \
    "ds_read_b64_tr_b16 a[128:129], v18 offset:24576\n\t" \
    "ds_read_b64_tr_b16 a[130:131], v18 offset:25088\n\t" \
    "ds_read_b64_tr_b16 a[132:133], v18 offset:25600\n\t" \
    "s_waitcnt lgkmcnt(7)\n\t" \
    "ds_read_b64_tr_b16 a[134:135], v18 offset:26112\n\t" \
    "ds_read_b64_tr_b16 a[136:137], v18 offset:26624\n\t" \
    "ds_read_b64_tr_b16 a[138:139], v18 offset:27136\n\t" \
    "ds_read_b64_tr_b16 a[140:141], v18 offset:27648\n\t" \
    "ds_read_b64_tr_b16 a[142:143], v18 offset:28160\n\t" \
    "ds_read_b64_tr_b16 a[144:145], v126 offset:28672\n\t" \
    "ds_read_b64_tr_b16 a[146:147], v126 offset:29184\n\t" \
    "ds_read_b64_tr_b16 a[148:149], v126 offset:29696\n\t" \
    "s_waitcnt lgkmcnt(7)\n\t" \
    "ds_read_b64_tr_b16 a[150:151], v126 offset:30208\n\t" \
    "ds_read_b64_tr_b16 a[152:153], v126 offset:30720\n\t" \
    "ds_read_b64_tr_b16 a[154:155], v126 offset:31232\n\t" \
    "ds_read_b64_tr_b16 a[156:157], v126 offset:31744\n\t" \
    "ds_read_b64_tr_b16 a[158:159], v126 offset:32256\n\t" \
    "s_waitcnt vmcnt(31)\n\t" \
    "ds_write_b128 v15, v[160:163] offset:16384\n\t" \
    "s_waitcnt vmcnt(30)\n\t" \
    "ds_write_b128 v15, v[164:167] offset:16640\n\t" \
    "s_waitcnt vmcnt(29)\n\t" \
    "ds_write_b128 v15, v[176:179] offset:18432\n\t" \
    "s_waitcnt vmcnt(28)\n\t" \
    "s_waitcnt lgkmcnt(7)\n\t" \
    "ds_write_b128 v15, v[180:183] offset:18688\n\t" \
    "s_waitcnt vmcnt(27)\n\t" \
    "ds_write_b128 v15, v[52:55] offset:20480\n\t" \
    "s_waitcnt vmcnt(26)\n\t" \
    "ds_write_b128 v15, v[56:59] offset:20736\n\t" \
    "s_waitcnt vmcnt(25)\n\t" \
    "ds_write_b128 v15, v[60:63] offset:22528\n\t" \
    "s_waitcnt vmcnt(24)\n\t" \
    "ds_write_b128 v15, v[64:67] offset:22784\n\t" \
    "s_add_u32 s75, s61, 524288\n\t" \
    "buffer_load_dwordx4 v[200:203], v14, %[rk], s75 offen\n\t" \
    "s_add_u32 s75, s61, 655360\n\t" \
    "buffer_load_dwordx4 v[204:207], v14, %[rk], s75 offen\n\t" \
    "s_add_u32 s75, s61, 786432\n\t" \
    "buffer_load_dwordx4 v[216:219], v14, %[rk], s75 offen\n\t" \
    "s_add_u32 s75, s61, 917504\n\t" \
    "buffer_load_dwordx4 v[220:223], v14, %[rk], s75 offen\n\t" \
    "s_add_u32 s75, s62, 524288\n\t" \
    "buffer_load_dwordx4 a[200:203], v14, %[rk], s75 offen\n\t" \
    "s_add_u32 s75, s62, 655360\n\t" \
    "buffer_load_dwordx4 a[204:207], v14, %[rk], s75 offen\n\t" \
    "s_add_u32 s75, s62, 786432\n\t" \
    "buffer_load_dwordx4 a[216:219], v14, %[rk], s75 offen\n\t" \
    "s_add_u32 s75, s62, 917504\n\t" \
    "buffer_load_dwordx4 a[220:223], v14, %[rk], s75 offen\n\t" \
    "ds_read_b64_tr_b16 v[160:161], v18 offset:16384\n\t" \
    "ds_read_b64_tr_b16 v[162:163], v18 offset:16896\n\t" \
    "ds_read_b64_tr_b16 v[164:165], v18 offset:18432\n\t" \
    "s_waitcnt lgkmcnt(7)\n\t" \
    "ds_read_b64_tr_b16 v[166:167], v18 offset:18944\n\t" \
    "ds_read_b64_tr_b16 v[52:53], v126 offset:17408\n\t" \
    "ds_read_b64_tr_b16 v[54:55], v126 offset:17920\n\t" \
    "ds_read_b64_tr_b16 v[56:57], v126 offset:19456\n\t" \
    "ds_read_b64_tr_b16 v[58:59], v126 offset:19968\n\t" \
    "ds_read_b64_tr_b16 v[176:177], v18 offset:20480\n\t" \
    "ds_read_b64_tr_b16 v[178:179], v18 offset:20992\n\t" \
    "ds_read_b64_tr_b16 v[180:181], v18 offset:22528\n\t" \
    "s_waitcnt lgkmcnt(7)\n\t" \
    "ds_read_b64_tr_b16 v[182:183], v18 offset:23040\n\t" \
    "ds_read_b64_tr_b16 v[60:61], v126 offset:21504\n\t" \
    "ds_read_b64_tr_b16 v[62:63], v126 offset:22016\n\t" \
    "ds_read_b64_tr_b16 v[64:65], v126 offset:23552\n\t" \
    "ds_read_b64_tr_b16 v[66:67], v126 offset:24064\n\t" \
    "s_waitcnt vmcnt(31)\n\t" \
    "ds_write_b128 v15, v[224:227] offset:24576\n\t" \
    "s_waitcnt vmcnt(30)\n\t" \
    "ds_write_b128 v15, v[228:231] offset:24832\n\t" \
    "s_waitcnt vmcnt(29)\n\t" \
    "ds_write_b128 v15, v[240:243] offset:26624\n\t" \
    "s_waitcnt vmcnt(28)\n\t" \
    "s_waitcnt lgkmcnt(7)\n\t" \
    "ds_write_b128 v15, v[244:247] offset:26880\n\t" \
    "s_waitcnt vmcnt(27)\n\t" \
    "ds_write_b128 v15, a[224:227] offset:28672\n\t" \
    "s_waitcnt vmcnt(26)\n\t" \
    "ds_write_b128 v15, a[228:231] offset:28928\n\t" \
    "s_waitcnt vmcnt(25)\n\t" \
    "ds_write_b128 v15, a[240:243] offset:30720\n\t" \
    "s_waitcnt vmcnt(24)\n\t" \
    "ds_write_b128 v15, a[244:247] offset:30976\n\t" \
    "ds_write_b128 %[wob], v[52:55] offset:8192\n\t" \
    "ds_write_b128 %[wob], v[56:59] offset:9216\n\t" \
    "s_waitcnt lgkmcnt(12)\n\t" \
    "ds_write_b128 %[wob], v[60:63] offset:12288\n\t" \
    "s_waitcnt lgkmcnt(11)\n\t" \
    "ds_write_b128 %[wob], v[64:67] offset:13312\n\t" \
    "s_add_u32 s75, s61, 524288\n\t" \
    "buffer_load_dwordx4 a[16:19], v14, %[rv], s75 offen\n\t" \
    "s_add_u32 s75, s61, 655360\n\t" \
    "buffer_load_dwordx4 a[20:23], v14, %[rv], s75 offen\n\t" \
    "s_add_u32 s75, s61, 786432\n\t" \
    "buffer_load_dwordx4 a[24:27], v14, %[rv], s75 offen\n\t" \
    "s_add_u32 s75, s61, 917504\n\t" \
    "buffer_load_dwordx4 a[28:31], v14, %[rv], s75 offen\n\t" \
    "s_add_u32 s75, s62, 524288\n\t" \
    "buffer_load_dwordx4 a[48:51], v14, %[rv], s75 offen\n\t" \
    "s_add_u32 s75, s62, 655360\n\t" \
    "buffer_load_dwordx4 a[52:55], v14, %[rv], s75 offen\n\t" \
    "s_add_u32 s75, s62, 786432\n\t" \
    "buffer_load_dwordx4 a[56:59], v14, %[rv], s75 offen\n\t" \
    "s_add_u32 s75, s62, 917504\n\t" \
    "buffer_load_dwordx4 a[60:63], v14, %[rv], s75 offen\n\t" \
    "ds_read_b64_tr_b16 v[224:225], v18 offset:24576\n\t" \
    "ds_read_b64_tr_b16 v[226:227], v18 offset:25088\n\t" \
    "ds_read_b64_tr_b16 v[228:229], v18 offset:26624\n\t" \
    "s_waitcnt lgkmcnt(7)\n\t" \
    "ds_read_b64_tr_b16 v[230:231], v18 offset:27136\n\t" \
    "ds_read_b64_tr_b16 a[224:225], v126 offset:25600\n\t" \
    "ds_read_b64_tr_b16 a[226:227], v126 offset:26112\n\t" \
    "ds_read_b64_tr_b16 a[228:229], v126 offset:27648\n\t" \
    "ds_read_b64_tr_b16 a[230:231], v126 offset:28160\n\t" \
    "ds_read_b64_tr_b16 v[240:241], v18 offset:28672\n\t" \
    "ds_read_b64_tr_b16 v[242:243], v18 offset:29184\n\t" \
    "ds_read_b64_tr_b16 v[244:245], v18 offset:30720\n\t" \
    "s_waitcnt lgkmcnt(7)\n\t" \
    "ds_read_b64_tr_b16 v[246:247], v18 offset:31232\n\t" \
    "ds_read_b64_tr_b16 a[240:241], v126 offset:29696\n\t" \
    "ds_read_b64_tr_b16 a[242:243], v126 offset:30208\n\t" \
    "ds_read_b64_tr_b16 a[244:245], v126 offset:31744\n\t" \
    "ds_read_b64_tr_b16 a[246:247], v126 offset:32256\n\t" \
    "s_waitcnt vmcnt(31)\n\t" \
    "ds_write_b128 v15, a[64:67] offset:16384\n\t" \
    "s_waitcnt vmcnt(30)\n\t" \
    "ds_write_b128 v15, a[68:71] offset:16640\n\t" \
    "s_waitcnt vmcnt(29)\n\t" \
    "ds_write_b128 v15, a[72:75] offset:18432\n\t" \
    "s_waitcnt vmcnt(28)\n\t" \
    "s_waitcnt lgkmcnt(7)\n\t" \
    "ds_write_b128 v15, a[76:79] offset:18688\n\t" \
    "s_waitcnt vmcnt(27)\n\t" \
    "ds_write_b128 v15, a[96:99] offset:20480\n\t" \
    "s_waitcnt vmcnt(26)\n\t" \
    "ds_write_b128 v15, a[100:103] offset:20736\n\t" \
    "s_waitcnt vmcnt(25)\n\t" \
    "ds_write_b128 v15, a[104:107] offset:22528\n\t" \
    "s_waitcnt vmcnt(24)\n\t" \
    "ds_write_b128 v15, a[108:111] offset:22784\n\t" \
    "s_mov_b32 s75, 65536\n\t" \
    "buffer_load_dwordx4 a[160:163], v16, %[ro], s75 offen\n\t" \
    "s_mov_b32 s75, 73728\n\t" \
    "buffer_load_dwordx4 a[164:167], v16, %[ro], s75 offen\n\t" \
    "s_mov_b32 s75, 81920\n\t" \
    "buffer_load_dwordx4 a[168:171], v16, %[ro], s75 offen\n\t" \
    "s_mov_b32 s75, 90112\n\t" \
    "buffer_load_dwordx4 a[172:175], v16, %[ro], s75 offen\n\t" \
    "s_mov_b32 s75, 98304\n\t" \
    "buffer_load_dwordx4 a[176:179], v16, %[ro], s75 offen\n\t" \
    "s_mov_b32 s75, 106496\n\t" \
    "buffer_load_dwordx4 a[180:183], v16, %[ro], s75 offen\n\t" \
    "s_mov_b32 s75, 114688\n\t" \
    "buffer_load_dwordx4 a[184:187], v16, %[ro], s75 offen\n\t" \
    "s_mov_b32 s75, 122880\n\t" \
    "buffer_load_dwordx4 a[188:191], v16, %[ro], s75 offen\n\t" \
    "ds_read_b64_tr_b16 a[64:65], v18 offset:16384\n\t" \
    "ds_read_b64_tr_b16 a[66:67], v18 offset:16896\n\t" \
    "ds_read_b64_tr_b16 a[68:69], v18 offset:18432\n\t" \
    "s_waitcnt lgkmcnt(7)\n\t" \
    "ds_read_b64_tr_b16 a[70:71], v18 offset:18944\n\t" \
    "ds_read_b64_tr_b16 a[72:73], v126 offset:17408\n\t" \
    "ds_read_b64_tr_b16 a[74:75], v126 offset:17920\n\t" \
    "ds_read_b64_tr_b16 a[76:77], v126 offset:19456\n\t" \
    "ds_read_b64_tr_b16 a[78:79], v126 offset:19968\n\t" \
    "ds_read_b64_tr_b16 a[96:97], v18 offset:20480\n\t" \
    "ds_read_b64_tr_b16 a[98:99], v18 offset:20992\n\t" \
    "ds_read_b64_tr_b16 a[100:101], v18 offset:22528\n\t" \
    "s_waitcnt lgkmcnt(7)\n\t" \
    "ds_read_b64_tr_b16 a[102:103], v18 offset:23040\n\t" \
    "ds_read_b64_tr_b16 a[104:105], v126 offset:21504\n\t" \
    "ds_read_b64_tr_b16 a[106:107], v126 offset:22016\n\t" \
    "ds_read_b64_tr_b16 a[108:109], v126 offset:23552\n\t" \
    "ds_read_b64_tr_b16 a[110:111], v126 offset:24064\n\t" \
    "s_waitcnt vmcnt(31)\n\t" \
    "ds_write_b128 v15, v[136:139] offset:24576\n\t" \
    "s_waitcnt vmcnt(30)\n\t" \
    "ds_write_b128 v15, v[140:143] offset:24832\n\t" \
    "s_waitcnt vmcnt(29)\n\t" \
    "ds_write_b128 v15, v[152:155] offset:26624\n\t" \
    "s_waitcnt vmcnt(28)\n\t" \
    "s_waitcnt lgkmcnt(7)\n\t" \
    "ds_write_b128 v15, v[156:159] offset:26880\n\t" \
    "s_waitcnt vmcnt(27)\n\t" \
    "ds_write_b128 v15, v[36:39] offset:28672\n\t" \
    "s_waitcnt vmcnt(26)\n\t" \
    "ds_write_b128 v15, v[40:43] offset:28928\n\t" \
    "s_waitcnt vmcnt(25)\n\t" \
    "ds_write_b128 v15, v[44:47] offset:30720\n\t" \
    "s_waitcnt vmcnt(24)\n\t" \
    "ds_write_b128 v15, v[48:51] offset:30976\n\t" \
    "s_add_u32 s75, s63, 524288\n\t" \
    "buffer_load_dwordx4 v[168:171], v14, %[rq], s75 offen\n\t" \
    "s_add_u32 s75, s63, 655360\n\t" \
    "buffer_load_dwordx4 v[172:175], v14, %[rq], s75 offen\n\t" \
    "s_add_u32 s75, s63, 786432\n\t" \
    "buffer_load_dwordx4 v[184:187], v14, %[rq], s75 offen\n\t" \
    "s_add_u32 s75, s63, 917504\n\t" \
    "buffer_load_dwordx4 v[188:191], v14, %[rq], s75 offen\n\t" \
    "s_add_u32 s75, s64, 524288\n\t" \
    "buffer_load_dwordx4 v[68:71], v14, %[rq], s75 offen\n\t" \
    "s_add_u32 s75, s64, 655360\n\t" \
    "buffer_load_dwordx4 v[72:75], v14, %[rq], s75 offen\n\t" \
    "s_add_u32 s75, s64, 786432\n\t" \
    "buffer_load_dwordx4 v[76:79], v14, %[rq], s75 offen\n\t" \
    "s_add_u32 s75, s64, 917504\n\t" \
    "buffer_load_dwordx4 v[80:83], v14, %[rq], s75 offen\n\t" \
    "ds_read_b64_tr_b16 v[136:137], v18 offset:24576\n\t" \
    "ds_read_b64_tr_b16 v[138:139], v18 offset:25088\n\t" \
    "ds_read_b64_tr_b16 v[140:141], v18 offset:26624\n\t" \
    "s_waitcnt lgkmcnt(7)\n\t" \
    "ds_read_b64_tr_b16 v[142:143], v18 offset:27136\n\t" \
    "ds_read_b64_tr_b16 v[36:37], v126 offset:25600\n\t" \
    "ds_read_b64_tr_b16 v[38:39], v126 offset:26112\n\t" \
    "ds_read_b64_tr_b16 v[40:41], v126 offset:27648\n\t" \
    "ds_read_b64_tr_b16 v[42:43], v126 offset:28160\n\t" \
    "ds_read_b64_tr_b16 v[152:153], v18 offset:28672\n\t" \
    "ds_read_b64_tr_b16 v[154:155], v18 offset:29184\n\t" \
    "ds_read_b64_tr_b16 v[156:157], v18 offset:30720\n\t" \
    "s_waitcnt lgkmcnt(7)\n\t" \
    "ds_read_b64_tr_b16 v[158:159], v18 offset:31232\n\t" \
    "ds_read_b64_tr_b16 v[44:45], v126 offset:29696\n\t" \
    "ds_read_b64_tr_b16 v[46:47], v126 offset:30208\n\t" \
    "ds_read_b64_tr_b16 v[48:49], v126 offset:31744\n\t" \
    "ds_read_b64_tr_b16 v[50:51], v126 offset:32256\n\t" \
    "s_waitcnt vmcnt(31)\n\t" \
    "ds_write_b128 v15, v[200:203] offset:16384\n\t" \
    "s_waitcnt vmcnt(30)\n\t" \
    "ds_write_b128 v15, v[204:207] offset:16640\n\t" \
    "s_waitcnt vmcnt(29)\n\t" \
    "ds_write_b128 v15, v[216:219] offset:18432\n\t" \
    "s_waitcnt vmcnt(28)\n\t" \
    "s_waitcnt lgkmcnt(7)\n\t" \
    "ds_write_b128 v15, v[220:223] offset:18688\n\t" \
    "s_waitcnt vmcnt(27)\n\t" \
    "ds_write_b128 v15, a[200:203] offset:20480\n\t" \
    "s_waitcnt vmcnt(26)\n\t" \
    "ds_write_b128 v15, a[204:207] offset:20736\n\t" \
    "s_waitcnt vmcnt(25)\n\t" \
    "ds_write_b128 v15, a[216:219] offset:22528\n\t" \
    "s_waitcnt vmcnt(24)\n\t" \
    "ds_write_b128 v15, a[220:223] offset:22784\n\t" \
    "ds_write_b128 %[wob], v[36:39] offset:2048\n\t" \
    "ds_write_b128 %[wob], v[40:43] offset:3072\n\t" \
    "s_waitcnt lgkmcnt(12)\n\t" \
    "ds_write_b128 %[wob], v[44:47] offset:6144\n\t" \
    "s_waitcnt lgkmcnt(11)\n\t" \
    "ds_write_b128 %[wob], v[48:51] offset:7168\n\t" \
    "s_add_u32 s75, s63, 524288\n\t" \
    "buffer_load_dwordx4 v[232:235], v14, %[rk], s75 offen\n\t" \
    "s_add_u32 s75, s63, 655360\n\t" \
    "buffer_load_dwordx4 v[236:239], v14, %[rk], s75 offen\n\t" \
    "s_add_u32 s75, s63, 786432\n\t" \
    "buffer_load_dwordx4 v[248:251], v14, %[rk], s75 offen\n\t" \
    "s_add_u32 s75, s63, 917504\n\t" \
    "buffer_load_dwordx4 v[252:255], v14, %[rk], s75 offen\n\t" \
    "s_add_u32 s75, s64, 524288\n\t" \
    "buffer_load_dwordx4 a[232:235], v14, %[rk], s75 offen\n\t" \
    "s_add_u32 s75, s64, 655360\n\t" \
    "buffer_load_dwordx4 a[236:239], v14, %[rk], s75 offen\n\t" \
    "s_add_u32 s75, s64, 786432\n\t" \
    "buffer_load_dwordx4 a[248:251], v14, %[rk], s75 offen\n\t" \
    "s_add_u32 s75, s64, 917504\n\t" \
    "buffer_load_dwordx4 a[252:255], v14, %[rk], s75 offen\n\t" \
    "ds_read_b64_tr_b16 v[200:201], v18 offset:16384\n\t" \
    "ds_read_b64_tr_b16 v[202:203], v18 offset:16896\n\t" \
    "ds_read_b64_tr_b16 v[204:205], v18 offset:18432\n\t" \
    "s_waitcnt lgkmcnt(7)\n\t" \
    "ds_read_b64_tr_b16 v[206:207], v18 offset:18944\n\t" \
    "ds_read_b64_tr_b16 a[200:201], v126 offset:17408\n\t" \
    "ds_read_b64_tr_b16 a[202:203], v126 offset:17920\n\t" \
    "ds_read_b64_tr_b16 a[204:205], v126 offset:19456\n\t" \
    "ds_read_b64_tr_b16 a[206:207], v126 offset:19968\n\t" \
    "ds_read_b64_tr_b16 v[216:217], v18 offset:20480\n\t" \
    "ds_read_b64_tr_b16 v[218:219], v18 offset:20992\n\t" \
    "ds_read_b64_tr_b16 v[220:221], v18 offset:22528\n\t" \
    "s_waitcnt lgkmcnt(7)\n\t" \
    "ds_read_b64_tr_b16 v[222:223], v18 offset:23040\n\t" \
    "ds_read_b64_tr_b16 a[216:217], v126 offset:21504\n\t" \
    "ds_read_b64_tr_b16 a[218:219], v126 offset:22016\n\t" \
    "ds_read_b64_tr_b16 a[220:221], v126 offset:23552\n\t" \
    "ds_read_b64_tr_b16 a[222:223], v126 offset:24064\n\t" \
    "s_waitcnt vmcnt(31)\n\t" \
    "ds_write_b128 v15, a[16:19] offset:24576\n\t" \
    "s_waitcnt vmcnt(30)\n\t" \
    "ds_write_b128 v15, a[20:23] offset:24832\n\t" \
    "s_waitcnt vmcnt(29)\n\t" \
    "ds_write_b128 v15, a[24:27] offset:26624\n\t" \
    "s_waitcnt vmcnt(28)\n\t" \
    "s_waitcnt lgkmcnt(7)\n\t" \
    "ds_write_b128 v15, a[28:31] offset:26880\n\t" \
    "s_waitcnt vmcnt(27)\n\t" \
    "ds_write_b128 v15, a[48:51] offset:28672\n\t" \
    "s_waitcnt vmcnt(26)\n\t" \
    "ds_write_b128 v15, a[52:55] offset:28928\n\t" \
    "s_waitcnt vmcnt(25)\n\t" \
    "ds_write_b128 v15, a[56:59] offset:30720\n\t" \
    "s_waitcnt vmcnt(24)\n\t" \
    "ds_write_b128 v15, a[60:63] offset:30976\n\t" \
    "s_add_u32 s75, s63, 524288\n\t" \
    "buffer_load_dwordx4 a[80:83], v14, %[rv], s75 offen\n\t" \
    "s_add_u32 s75, s63, 655360\n\t" \
    "buffer_load_dwordx4 a[84:87], v14, %[rv], s75 offen\n\t" \
    "s_add_u32 s75, s63, 786432\n\t" \
    "buffer_load_dwordx4 a[88:91], v14, %[rv], s75 offen\n\t" \
    "s_add_u32 s75, s63, 917504\n\t" \
    "buffer_load_dwordx4 a[92:95], v14, %[rv], s75 offen\n\t" \
    "s_add_u32 s75, s64, 524288\n\t" \
    "buffer_load_dwordx4 a[112:115], v14, %[rv], s75 offen\n\t" \
    "s_add_u32 s75, s64, 655360\n\t" \
    "buffer_load_dwordx4 a[116:119], v14, %[rv], s75 offen\n\t" \
    "s_add_u32 s75, s64, 786432\n\t" \
    "buffer_load_dwordx4 a[120:123], v14, %[rv], s75 offen\n\t" \
    "s_add_u32 s75, s64, 917504\n\t" \
    "buffer_load_dwordx4 a[124:127], v14, %[rv], s75 offen\n\t" \
    "ds_read_b64_tr_b16 a[16:17], v18 offset:24576\n\t" \
    "ds_read_b64_tr_b16 a[18:19], v18 offset:25088\n\t" \
    "ds_read_b64_tr_b16 a[20:21], v18 offset:26624\n\t" \
    "s_waitcnt lgkmcnt(7)\n\t" \
    "ds_read_b64_tr_b16 a[22:23], v18 offset:27136\n\t" \
    "ds_read_b64_tr_b16 a[24:25], v126 offset:25600\n\t" \
    "ds_read_b64_tr_b16 a[26:27], v126 offset:26112\n\t" \
    "ds_read_b64_tr_b16 a[28:29], v126 offset:27648\n\t" \
    "ds_read_b64_tr_b16 a[30:31], v126 offset:28160\n\t" \
    "ds_read_b64_tr_b16 a[48:49], v18 offset:28672\n\t" \
    "ds_read_b64_tr_b16 a[50:51], v18 offset:29184\n\t" \
    "ds_read_b64_tr_b16 a[52:53], v18 offset:30720\n\t" \
    "s_waitcnt lgkmcnt(7)\n\t" \
    "ds_read_b64_tr_b16 a[54:55], v18 offset:31232\n\t" \
    "ds_read_b64_tr_b16 a[56:57], v126 offset:29696\n\t" \
    "ds_read_b64_tr_b16 a[58:59], v126 offset:30208\n\t" \
    "ds_read_b64_tr_b16 a[60:61], v126 offset:31744\n\t" \
    "ds_read_b64_tr_b16 a[62:63], v126 offset:32256\n\t" \
    "s_waitcnt vmcnt(31)\n\t" \
    "ds_write_b64 v17, a[160:161] offset:16384\n\t" \
    "ds_write_b64 v17, a[162:163] offset:18432\n\t" \
    "s_waitcnt vmcnt(30)\n\t" \
    "ds_write_b64 v17, a[164:165] offset:17408\n\t" \
    "s_waitcnt lgkmcnt(7)\n\t" \
    "ds_write_b64 v17, a[166:167] offset:19456\n\t" \
    "s_waitcnt vmcnt(29)\n\t" \
    "ds_write_b64 v17, a[168:169] offset:16512\n\t" \
    "ds_write_b64 v17, a[170:171] offset:18560\n\t" \
    "s_waitcnt vmcnt(28)\n\t" \
    "ds_write_b64 v17, a[172:173] offset:17536\n\t" \
    "ds_write_b64 v17, a[174:175] offset:19584\n\t" \
    "s_waitcnt vmcnt(27)\n\t" \
    "ds_write_b64 v125, a[176:177] offset:16384\n\t" \
    "ds_write_b64 v125, a[178:179] offset:18432\n\t" \
    "s_waitcnt vmcnt(26)\n\t" \
    "ds_write_b64 v125, a[180:181] offset:17408\n\t" \
    "s_waitcnt lgkmcnt(7)\n\t" \
    "ds_write_b64 v125, a[182:183] offset:19456\n\t" \
    "s_waitcnt vmcnt(25)\n\t" \
    "ds_write_b64 v125, a[184:185] offset:16512\n\t" \
    "ds_write_b64 v125, a[186:187] offset:18560\n\t" \
    "s_waitcnt vmcnt(24)\n\t" \
    "ds_write_b64 v125, a[188:189] offset:17536\n\t" \
    "ds_write_b64 v125, a[190:191] offset:19584\n\t" \
    "buffer_load_dwordx2 v[112:113], v114, %[rs], s76 offen sc1\n\t" \
    "ds_read_b64_tr_b16 a[160:161], v18 offset:16384\n\t" \
    "ds_read_b64_tr_b16 a[162:163], v18 offset:16896\n\t" \
    "ds_read_b64_tr_b16 a[164:165], v18 offset:17408\n\t" \
    "s_waitcnt lgkmcnt(7)\n\t" \
    "ds_read_b64_tr_b16 a[166:167], v18 offset:17920\n\t" \
    "ds_read_b64_tr_b16 a[168:169], v18 offset:18432\n\t" \
    "ds_read_b64_tr_b16 a[170:171], v18 offset:18944\n\t" \
    "ds_read_b64_tr_b16 a[172:173], v18 offset:19456\n\t" \
    "ds_read_b64_tr_b16 a[174:175], v18 offset:19968\n\t" \
    "ds_read_b64_tr_b16 a[176:177], v126 offset:20480\n\t" \
    "ds_read_b64_tr_b16 a[178:179], v126 offset:20992\n\t" \
    "ds_read_b64_tr_b16 a[180:181], v126 offset:21504\n\t" \
    "s_waitcnt lgkmcnt(7)\n\t" \
    "ds_read_b64_tr_b16 a[182:183], v126 offset:22016\n\t" \
    "ds_read_b64_tr_b16 a[184:185], v126 offset:22528\n\t" \
    "ds_read_b64_tr_b16 a[186:187], v126 offset:23040\n\t" \
    "ds_read_b64_tr_b16 a[188:189], v126 offset:23552\n\t" \
    "ds_read_b64_tr_b16 a[190:191], v126 offset:24064\n\t" \
    "s_waitcnt vmcnt(24)\n\t" \
    "ds_write_b128 v15, v[168:171] offset:24576\n\t" \
    "s_waitcnt vmcnt(23)\n\t" \
    "ds_write_b128 v15, v[172:175] offset:24832\n\t" \
    "s_waitcnt vmcnt(22)\n\t" \
    "ds_write_b128 v15, v[184:187] offset:26624\n\t" \
    "s_waitcnt vmcnt(21)\n\t" \
    "s_waitcnt lgkmcnt(7)\n\t" \
    "ds_write_b128 v15, v[188:191] offset:26880\n\t" \
    "s_waitcnt vmcnt(20)\n\t" \
    "ds_write_b128 v15, v[68:71] offset:28672\n\t" \
    "s_waitcnt vmcnt(19)\n\t" \
    "ds_write_b128 v15, v[72:75] offset:28928\n\t" \
    "s_waitcnt vmcnt(18)\n\t" \
    "ds_write_b128 v15, v[76:79] offset:30720\n\t" \
    "s_waitcnt vmcnt(17)\n\t" \
    "ds_write_b128 v15, v[80:83] offset:30976\n\t" \
    "ds_read_b64_tr_b16 v[168:169], v18 offset:24576\n\t" \
    "ds_read_b64_tr_b16 v[170:171], v18 offset:25088\n\t" \
    "ds_read_b64_tr_b16 v[172:173], v18 offset:26624\n\t" \
    "s_waitcnt lgkmcnt(7)\n\t" \
    "ds_read_b64_tr_b16 v[174:175], v18 offset:27136\n\t" \
    "ds_read_b64_tr_b16 v[68:69], v126 offset:25600\n\t" \
    "ds_read_b64_tr_b16 v[70:71], v126 offset:26112\n\t" \
    "ds_read_b64_tr_b16 v[72:73], v126 offset:27648\n\t" \
    "ds_read_b64_tr_b16 v[74:75], v126 offset:28160\n\t" \
    "ds_read_b64_tr_b16 v[184:185], v18 offset:28672\n\t" \
    "ds_read_b64_tr_b16 v[186:187], v18 offset:29184\n\t" \
    "ds_read_b64_tr_b16 v[188:189], v18 offset:30720\n\t" \
    "s_waitcnt lgkmcnt(7)\n\t" \
    "ds_read_b64_tr_b16 v[190:191], v18 offset:31232\n\t" \
    "ds_read_b64_tr_b16 v[76:77], v126 offset:29696\n\t" \
    "ds_read_b64_tr_b16 v[78:79], v126 offset:30208\n\t" \
    "ds_read_b64_tr_b16 v[80:81], v126 offset:31744\n\t" \
    "ds_read_b64_tr_b16 v[82:83], v126 offset:32256\n\t" \
    "s_waitcnt vmcnt(16)\n\t" \
    "ds_write_b128 v15, v[232:235] offset:16384\n\t" \
    "s_waitcnt vmcnt(15)\n\t" \
    "ds_write_b128 v15, v[236:239] offset:16640\n\t" \
    "s_waitcnt vmcnt(14)\n\t" \
    "ds_write_b128 v15, v[248:251] offset:18432\n\t" \
    "s_waitcnt vmcnt(13)\n\t" \
    "s_waitcnt lgkmcnt(7)\n\t" \
    "ds_write_b128 v15, v[252:255] offset:18688\n\t" \
    "s_waitcnt vmcnt(12)\n\t" \
    "ds_write_b128 v15, a[232:235] offset:20480\n\t" \
    "s_waitcnt vmcnt(11)\n\t" \
    "ds_write_b128 v15, a[236:239] offset:20736\n\t" \
    "s_waitcnt vmcnt(10)\n\t" \
    "ds_write_b128 v15, a[248:251] offset:22528\n\t" \
    "s_waitcnt vmcnt(9)\n\t" \
    "ds_write_b128 v15, a[252:255] offset:22784\n\t" \
    "ds_write_b128 %[wob], v[68:71] offset:10240\n\t" \
    "ds_write_b128 %[wob], v[72:75] offset:11264\n\t" \
    "s_waitcnt lgkmcnt(12)\n\t" \
    "ds_write_b128 %[wob], v[76:79] offset:14336\n\t" \
    "s_waitcnt lgkmcnt(11)\n\t" \
    "ds_write_b128 %[wob], v[80:83] offset:15360\n\t" \
    "ds_read_b64_tr_b16 v[232:233], v18 offset:16384\n\t" \
    "ds_read_b64_tr_b16 v[234:235], v18 offset:16896\n\t" \
    "ds_read_b64_tr_b16 v[236:237], v18 offset:18432\n\t" \
    "s_waitcnt lgkmcnt(7)\n\t" \
    "ds_read_b64_tr_b16 v[238:239], v18 offset:18944\n\t" \
    "ds_read_b64_tr_b16 a[232:233], v126 offset:17408\n\t" \
    "ds_read_b64_tr_b16 a[234:235], v126 offset:17920\n\t" \
    "ds_read_b64_tr_b16 a[236:237], v126 offset:19456\n\t" \
    "ds_read_b64_tr_b16 a[238:239], v126 offset:19968\n\t" \
    "ds_read_b64_tr_b16 v[248:249], v18 offset:20480\n\t" \
    "ds_read_b64_tr_b16 v[250:251], v18 offset:20992\n\t" \
    "ds_read_b64_tr_b16 v[252:253], v18 offset:22528\n\t" \
    "s_waitcnt lgkmcnt(7)\n\t" \
    "ds_read_b64_tr_b16 v[254:255], v18 offset:23040\n\t" \
    "ds_read_b64_tr_b16 a[248:249], v126 offset:21504\n\t" \
    "ds_read_b64_tr_b16 a[250:251], v126 offset:22016\n\t" \
    "ds_read_b64_tr_b16 a[252:253], v126 offset:23552\n\t" \
    "ds_read_b64_tr_b16 a[254:255], v126 offset:24064\n\t" \
    "v_readfirstlane_b32 s80, v124\n\t" \
    "s_nop 3\n\t" \
    "s_add_u32 s80, s80, 1\n\t" \
    "s_mov_b32 s74, 0\n\t" \
    "s_mov_b32 s69, %[skv]\n\t" \
    "s_waitcnt vmcnt(0)\n\t" \
    "LIN_%=:\n\t" \
    "v_cmp_ne_u32 vcc, s80, v113\n\t" \
    "v_subrev_u32 v115, 2, v112\n\t" \
    "v_cmp_lt_u32 s[82:83], 62, v115\n\t" \
    "v_readfirstlane_b32 s65, v112\n\t" \
    "s_or_b64 vcc, vcc, s[82:83]\n\t" \
    "s_cbranch_scc1 LINS_%=\n\t" \
    "LINK_%=:\n\t" \
    "buffer_load_dwordx4 v[84:87], %[l16], %[rs], s69 offen sc1\n\t" \
    "buffer_load_dwordx4 v[88:91], %[l16], %[rs], s69 offen offset:1024 sc1\n\t" \
    "buffer_load_dwordx4 v[92:95], %[l16], %[rs], s69 offen offset:2048 sc1\n\t" \
    "buffer_load_dwordx4 v[96:99], %[l16], %[rs], s69 offen offset:3072 sc1\n\t" \
    "ds_write_b128 v15, a[80:83] offset:24576\n\t" \
    "ds_write_b128 v15, a[84:87] offset:24832\n\t" \
    "ds_write_b128 v15, a[88:91] offset:26624\n\t" \
    "s_waitcnt lgkmcnt(7)\n\t" \
    "ds_write_b128 v15, a[92:95] offset:26880\n\t" \
    "ds_write_b128 v15, a[112:115] offset:28672\n\t" \
    "ds_write_b128 v15, a[116:119] offset:28928\n\t" \
    "ds_write_b128 v15, a[120:123] offset:30720\n\t" \
    "ds_write_b128 v15, a[124:127] offset:30976\n\t" \
    "ds_read_b64_tr_b16 a[80:81], v18 offset:24576\n\t" \
    "ds_read_b64_tr_b16 a[82:83], v18 offset:25088\n\t" \
    "ds_read_b64_tr_b16 a[84:85], v18 offset:26624\n\t" \
    "s_waitcnt lgkmcnt(7)\n\t" \
    "ds_read_b64_tr_b16 a[86:87], v18 offset:27136\n\t" \
    "ds_read_b64_tr_b16 a[88:89], v126 offset:25600\n\t" \
    "ds_read_b64_tr_b16 a[90:91], v126 offset:26112\n\t" \
    "ds_read_b64_tr_b16 a[92:93], v126 offset:27648\n\t" \
    "ds_read_b64_tr_b16 a[94:95], v126 offset:28160\n\t" \
    "ds_read_b64_tr_b16 a[112:113], v18 offset:28672\n\t" \
    "ds_read_b64_tr_b16 a[114:115], v18 offset:29184\n\t" \
    "ds_read_b64_tr_b16 a[116:117], v18 offset:30720\n\t" \
    "s_waitcnt lgkmcnt(7)\n\t" \
    "ds_read_b64_tr_b16 a[118:119], v18 offset:31232\n\t" \
    "ds_read_b64_tr_b16 a[120:121], v126 offset:29696\n\t" \
    "ds_read_b64_tr_b16 a[122:123], v126 offset:30208\n\t" \
    "ds_read_b64_tr_b16 a[124:125], v126 offset:31744\n\t" \
    "ds_read_b64_tr_b16 a[126:127], v126 offset:32256\n\t" \
    "s_waitcnt lgkmcnt(0)\n\t" \
    "s_barrier\n\t" \
    "v_mov_b32 v14, 0x3f803f80\n\t" \
    "v_mov_b32 v15, 0x3f803f80\n\t" \
    "v_mov_b32 v16, 0x3f803f80\n\t" \
    "v_mov_b32 v17, 0x3f803f80\n\t" \
    "v_mov_b32 v18, 0\n\t" \
    "s_mov_b32 s60, 0\n\t" \
    "s_mov_b32 s71, 2\n\t" \
    "s_mov_b32 s70, %[sflg]\n\t" \
    "s_mov_b32 s67, %[skm]\n\t" \
    "s_mov_b32 s72, %[skmf]\n\t" \
    "s_and_b32 s61, s65, 1\n\t" \
    "s_lshl_b32 s61, s61, 13\n\t" \
    "s_add_u32 s63, s61, %[srld]\n\t" \
    "s_lshl_b32 s75, %[wv], 10\n\t" \
    "s_add_u32 s64, s63, s75\n\t" \
    "s_add_u32 s64, s64, 0x80000\n\t" \
    "s_add_u32 s65, s61, 0x4000\n\t" \
    "s_add_u32 s68, s61, 0x100000\n\t" \
    "s_mov_b32 s81, 1\n\t" \
    "s_bfe_u32 s75, %[sca], 0x10006\n\t" \
    "s_cmp_eq_u32 %[wv], s75\n\t" \
    "s_cselect_b64 s[98:99], -1, 0\n\t" \
    "s_mov_b32 s86, 1850\n\t" \
    "s_mov_b32 s87, 2970\n\t" \
    "s_mov_b32 s62, 0\n\t" \
    "v_mov_b32 v20, v116\n\t" \
    "v_mov_b32 v21, v117\n\t" \
    "v_mov_b32 v22, v118\n\t" \
    "v_mov_b32 v23, v119\n\t" \
    "v_mov_b32 v24, v120\n\t" \
    "v_mov_b32 v25, v121\n\t" \
    "v_mov_b32 v26, v122\n\t" \
    "v_mov_b32 v27, v123\n\t" \
    "s_lshl_b32 s75, %[wv], 12\n\t" \
    "v_add_u32 v45, s75, %[l16]\n\t" \
    "s_waitcnt vmcnt(3)\n\t" \
    "ds_write_b128 v45, v[84:87] offset:8192\n\t" \
    "s_waitcnt vmcnt(2)\n\t" \
    "ds_write_b128 v45, v[88:91] offset:9216\n\t" \
    "s_waitcnt vmcnt(1)\n\t" \
    "ds_write_b128 v45, v[92:95] offset:10240\n\t" \
    "s_waitcnt vmcnt(0)\n\t" \
    "ds_write_b128 v45, v[96:99] offset:11264\n\t" \
    "s_add_u32 s69, s69, 0x40000\n\t" \
    "s_waitcnt lgkmcnt(0)\n\t" \
    "s_memtime s[84:85]\n\t" \
    "s_memrealtime s[40:41]\n\t" \
    "s_nop 2\n\t" \
    "s_memrealtime s[42:43]\n\t" \
    "s_nop 2\n\t" \
    "s_memrealtime s[44:45]\n\t" \
    "s_nop 2\n\t" \
    "s_memrealtime s[46:47]\n\t" \
    "s_nop 2\n\t" \
    "s_memrealtime s[48:49]\n\t" \
    "s_nop 2\n\t" \
    "s_memrealtime s[50:51]\n\t" \
    "s_nop 2\n\t" \
    "s_memrealtime s[52:53]\n\t" \
    "s_nop 2\n\t" \
    "s_waitcnt lgkmcnt(0)\n\t" \
    "s_mov_b32 s75, 0\n\t" \
    "s_sub_u32 s43, s42, s40\n\t" \
    "s_mul_i32 s43, s43, 23\n\t" \
    "s_sub_u32 s43, s43, 17\n\t" \
    "s_max_i32 s75, s75, s43\n\t" \
    "s_sub_u32 s45, s44, s40\n\t" \
    "s_mul_i32 s45, s45, 23\n\t" \
    "s_sub_u32 s45, s45, 34\n\t" \
    "s_max_i32 s75, s75, s45\n\t" \
    "s_sub_u32 s47, s46, s40\n\t" \
    "s_mul_i32 s47, s47, 23\n\t" \
    "s_sub_u32 s47, s47, 51\n\t" \
    "s_max_i32 s75, s75, s47\n\t" \
    "s_sub_u32 s49, s48, s40\n\t" \
    "s_mul_i32 s49, s49, 23\n\t" \
    "s_sub_u32 s49, s49, 68\n\t" \
    "s_max_i32 s75, s75, s49\n\t" \
    "s_sub_u32 s51, s50, s40\n\t" \
    "s_mul_i32 s51, s51, 23\n\t" \
    "s_sub_u32 s51, s51, 85\n\t" \
    "s_max_i32 s75, s75, s51\n\t" \
    "s_sub_u32 s53, s52, s40\n\t" \
    "s_mul_i32 s53, s53, 23\n\t" \
    "s_sub_u32 s53, s53, 102\n\t" \
    "s_max_i32 s75, s75, s53\n\t" \
    "s_mul_i32 s40, s40, 187\n\t" \
    "s_lshr_b32 s40, s40, 3\n\t" \
    "s_add_u32 s56, s40, s75\n\t" \
    "s_sub_u32 s56, s56, s84\n\t" \
    "s_waitcnt vmcnt(0) lgkmcnt(0)\n\t" \
    "ds_read_b128 v[76:79], %[wob]\n\t" \
    "ds_read_b128 v[80:83], %[wob] offset:1024\n\t" \
    "ds_read_b128 v[84:87], %[wob] offset:2048\n\t" \
    "ds_read_b128 v[88:91], %[wob] offset:3072\n\t" \
    "ds_read_b128 v[92:95], %[wob] offset:4096\n\t" \
    "ds_read_b128 v[96:99], %[wob] offset:5120\n\t" \
    "ds_read_b128 v[100:103], %[wob] offset:6144\n\t" \
    "ds_read_b128 v[104:107], %[wob] offset:7168\n\t" \
    "v_mov_b64 v[52:53], 0\n\t" \
    "v_mov_b64 v[54:55], 0\n\t" \
    "v_mov_b64 v[56:57], 0\n\t" \
    "v_mov_b64 v[58:59], 0\n\t" \
    "v_mov_b64 v[60:61], 0\n\t" \
    "v_mov_b64 v[62:63], 0\n\t" \
    "v_mov_b64 v[64:65], 0\n\t" \
    "v_mov_b64 v[66:67], 0\n\t" \
    "v_mov_b64 v[68:69], 0\n\t" \
    "v_mov_b64 v[70:71], 0\n\t" \
    "v_mov_b64 v[72:73], 0\n\t" \
    "v_mov_b64 v[74:75], 0\n\t" \
    "v_mov_b32 v124, 0xff81ff81\n\t" \
    "v_mov_b32 v125, 0xff81ff81\n\t" \
    "v_mov_b32 v126, 0xff81ff81\n\t" \
    "v_mov_b32 v127, 0xff81ff81\n\t" \
    "v_mov_b32 v120, s60\n\t" \
    "v_mov_b32 v121, s80\n\t" \
    "s_and_b32 s75, s60, 1\n\t" \
    "s_lshl_b32 s75, s75, 12\n\t" \
    "s_add_u32 s38, s75, s68\n\t" \
    "s_add_u32 s59, s38, %[sca]\n\t" \
    "s_lshl_b32 s75, s60, 26\n\t" \
    "s_or_b32 s62, s62, s75\n\t" \
    "LTOP_%=:\n\t" \
    "s_waitcnt vmcnt(3) lgkmcnt(0)\n\t" \
    "v_smfmac_f32_16x16x64_bf16 v[56:59], v[20:23], v[76:83], %[sidx]\n\t" \
    "ds_write_b128 %[mw], v[20:23]\n\t" \
    "v_max3_u32 v116, v20, v21, v22\n\t" \
    "s_waitcnt vmcnt(2)\n\t" \
    "v_smfmac_f32_16x16x64_bf16 v[52:55], v[20:23], v[128:135], %[sidx]\n\t" \
    "ds_write_b128 %[mw], v[24:27] offset:1024\n\t" \
    "v_max3_u32 v116, v23, v24, v116\n\t" \
    "v_smfmac_f32_16x16x64_bf16 v[56:59], v[24:27], v[84:91], %[sidx]\n\t" \
    "ds_read_b128 v[76:79], %[wob] offset:8192\n\t" \
    "v_max3_u32 v116, v25, v26, v116\n\t" \
    "v_smfmac_f32_16x16x64_bf16 v[60:63], v[20:23], v[192:199], %[sidx]\n\t" \
    "ds_read_b128 v[80:83], %[wob] offset:9216\n\t" \
    "v_max_u32 v116, v27, v116\n\t" \
    "v_smfmac_f32_16x16x64_bf16 v[64:67], v[20:23], a[192:199], %[sidx]\n\t" \
    "s_memtime s[84:85]\n\t" \
    "v_cmp_le_u32 vcc, 0xff810000, v116\n\t" \
    "v_smfmac_f32_16x16x64_bf16 v[68:71], v[20:23], a[0:7], %[sidx]\n\t" \
    "s_cbranch_vccnz LG1_%=\n\t" \
    "LGA_%=:\n\t" \
    "s_waitcnt lgkmcnt(0)\n\t" \
    "s_barrier\n\t" \
    "v_smfmac_f32_16x16x64_bf16 v[72:75], v[20:23], a[8:15], %[sidx]\n\t" \
    "ds_read_b128 v[28:31], %[mr1]\n\t" \
    "s_add_u32 s39, s84, s56\n\t" \
    "v_smfmac_f32_16x16x64_bf16 v[52:55], v[24:27], v[136:143], %[sidx]\n\t" \
    "ds_read_b128 v[32:35], %[mr1] offset:1024\n\t" \
    "s_and_b32 s39, s39, 0xffffff\n\t" \
    "v_smfmac_f32_16x16x64_bf16 v[60:63], v[24:27], v[200:207], %[sidx]\n\t" \
    "ds_read_b128 v[36:39], %[mr2]\n\t" \
    "s_or_b32 s39, s39, s62\n\t" \
    "v_smfmac_f32_16x16x64_bf16 v[64:67], v[24:27], a[200:207], %[sidx]\n\t" \
    "ds_read_b128 v[40:43], %[mr2] offset:1024\n\t" \
    "v_mov_b32 v122, s39\n\t" \
    "v_smfmac_f32_16x16x64_bf16 v[68:71], v[24:27], a[16:23], %[sidx]\n\t" \
    "ds_read_b128 v[44:47], %[mr3]\n\t" \
    "s_mov_b64 exec, 1\n\t" \
    "buffer_store_dword v122, off, %[rs], s59 sc1\n\t" \
    "s_mov_b64 exec, -1\n\t" \
    "v_smfmac_f32_16x16x64_bf16 v[72:75], v[24:27], a[24:31], %[sidx]\n\t" \
    "ds_read_b128 v[48:51], %[mr3] offset:1024\n\t" \
    "s_and_b32 s39, s39, 0xfcffffff\n\t" \
    "s_waitcnt lgkmcnt(5)\n\t" \
    "v_smfmac_f32_16x16x64_bf16 v[56:59], v[28:31], v[92:99], %[sidx]\n\t" \
    "ds_read_b128 v[84:87], %[wob] offset:10240\n\t" \
    "s_mov_b32 s57, s84\n\t" \
    "s_waitcnt lgkmcnt(5)\n\t" \
    "v_smfmac_f32_16x16x64_bf16 v[56:59], v[32:35], v[100:107], %[sidx]\n\t" \
    "ds_read_b128 v[88:91], %[wob] offset:11264\n\t" \
    "s_mov_b32 s62, 0\n\t" \
    "v_smfmac_f32_16x16x64_bf16 v[52:55], v[28:31], v[144:151], %[sidx]\n\t" \
    "buffer_store_dwordx4 v[124:127], %[l16], %[rs], s64 offen sc1\n\t" \
    "v_smfmac_f32_16x16x64_bf16 v[52:55], v[32:35], v[152:159], %[sidx]\n\t" \
    "ds_read_b128 v[92:95], %[wob] offset:12288\n\t" \
    "v_smfmac_f32_16x16x64_bf16 v[60:63], v[28:31], v[208:215], %[sidx]\n\t" \
    "ds_read_b128 v[96:99], %[wob] offset:13312\n\t" \
    "s_waitcnt lgkmcnt(7)\n\t" \
    "v_smfmac_f32_16x16x64_bf16 v[56:59], v[36:39], v[76:83], %[sidx]\n\t" \
    "ds_read_b128 v[100:103], %[wob] offset:14336\n\t" \
    "v_smfmac_f32_16x16x64_bf16 v[52:55], v[36:39], v[160:167], %[sidx]\n\t" \
    "buffer_load_dwordx4 v[108:111], %[l16], %[rs], s69 offen sc1\n\t" \
    "s_waitcnt lgkmcnt(7)\n\t" \
    "v_smfmac_f32_16x16x64_bf16 v[52:55], v[40:43], v[168:175], %[sidx]\n\t" \
    "ds_read_b128 v[104:107], %[wob] offset:15360\n\t" \
    "s_waitcnt lgkmcnt(7)\n\t" \
    "v_smfmac_f32_16x16x64_bf16 v[52:55], v[44:47], v[176:183], %[sidx]\n\t" \
    "s_mov_b64 exec, 1\n\t" \
    "buffer_store_dwordx2 v[120:121], off, %[rs], s72 sc1\n\t" \
    "s_mov_b64 exec, -1\n\t" \
    "s_waitcnt lgkmcnt(4)\n\t" \
    "v_smfmac_f32_16x16x64_bf16 v[56:59], v[40:43], v[84:91], %[sidx]\n\t" \
    "ds_read_b128 v[76:79], %[l16] offset:8192\n\t" \
    "v_smfmac_f32_16x16x64_bf16 v[52:55], v[48:51], v[184:191], %[sidx]\n\t" \
    "ds_read_b128 v[80:83], %[l16] offset:12288\n\t" \
    "s_waitcnt lgkmcnt(4)\n\t" \
    "v_smfmac_f32_16x16x64_bf16 v[56:59], v[44:47], v[92:99], %[sidx]\n\t" \
    "ds_read_b128 v[84:87], %[l16] offset:9216\n\t" \
    "v_smfmac_f32_16x16x64_bf16 v[60:63], v[32:35], v[216:223], %[sidx]\n\t" \
    "buffer_load_dwordx4 v[112:115], %[l16], %[rs], s69 offen offset:1024 sc1\n\t" \
    "v_add_f32 v52, v52, v54\n\t" \
    "s_waitcnt lgkmcnt(3)\n\t" \
    "v_smfmac_f32_16x16x64_bf16 v[56:59], v[48:51], v[100:107], %[sidx]\n\t" \
    "ds_read_b128 v[88:91], %[l16] offset:13312\n\t" \
    "v_add_f32 v53, v53, v55\n\t" \
    "v_smfmac_f32_16x16x64_bf16 v[60:63], v[36:39], v[224:231], %[sidx]\n\t" \
    "ds_read_b128 v[92:95], %[l16] offset:10240\n\t" \
    "v_mul_f32 v52, s78, v52\n\t" \
    "v_smfmac_f32_16x16x64_bf16 v[60:63], v[40:43], v[232:239], %[sidx]\n\t" \
    "ds_read_b128 v[96:99], %[l16] offset:14336\n\t" \
    "v_mul_f32 v53, s78, v53\n\t" \
    "v_smfmac_f32_16x16x64_bf16 v[60:63], v[44:47], v[240:247], %[sidx]\n\t" \
    "buffer_load_dwordx4 v[116:119], %[l16], %[rs], s69 offen offset:2048 sc1\n\t" \
    "v_cvt_pk_bf16_f32 v124, v52, v53\n\t" \
    "v_smfmac_f32_16x16x64_bf16 v[60:63], v[48:51], v[248:255], %[sidx]\n\t" \
    "ds_read_b128 v[100:103], %[l16] offset:11264\n\t" \
    "v_add_f32 v56, v56, v58\n\t" \
    "v_smfmac_f32_16x16x64_bf16 v[64:67], v[28:31], a[208:215], %[sidx]\n\t" \
    "ds_read_b128 v[104:107], %[l16] offset:15360\n\t" \
    "v_add_f32 v57, v57, v59\n\t" \
    "v_smfmac_f32_16x16x64_bf16 v[64:67], v[32:35], a[216:223], %[sidx]\n\t" \
    "s_waitcnt vmcnt(6)\n\t" \
    "v_mul_f32 v56, s78, v56\n\t" \
    "v_smfmac_f32_16x16x64_bf16 v[64:67], v[36:39], a[224:231], %[sidx]\n\t" \
    "buffer_load_dwordx4 v[120:123], %[l16], %[rs], s69 offen offset:3072 sc1\n\t" \
    "v_mul_f32 v57, s78, v57\n\t" \
    "v_smfmac_f32_16x16x64_bf16 v[64:67], v[40:43], a[232:239], %[sidx]\n\t" \
    "ds_read_b128 v[20:23], %[mw] offset:16384\n\t" \
    "v_cvt_pk_bf16_f32 v126, v56, v57\n\t" \
    "v_smfmac_f32_16x16x64_bf16 v[64:67], v[44:47], a[240:247], %[sidx]\n\t" \
    "ds_read_b128 v[24:27], %[mw] offset:17408\n\t" \
    "v_add_f32 v60, v60, v62\n\t" \
    "v_smfmac_f32_16x16x64_bf16 v[64:67], v[48:51], a[248:255], %[sidx]\n\t" \
    "v_add_f32 v61, v61, v63\n\t" \
    "v_smfmac_f32_16x16x64_bf16 v[68:71], v[28:31], a[32:39], %[sidx]\n\t" \
    "v_cvt_pk_bf16_f32 v125, v60, v61\n\t" \
    "v_smfmac_f32_16x16x64_bf16 v[72:75], v[28:31], a[40:47], %[sidx]\n\t" \
    "v_add_f32 v64, v64, v66\n\t" \
    "v_add_f32 v65, v65, v67\n\t" \
    "v_cvt_pk_bf16_f32 v127, v64, v65\n\t" \
    "ds_write2st64_b64 %[qkw], v[124:125], v[126:127] offset0:48 offset1:49\n\t" \
    "v_smfmac_f32_16x16x64_bf16 v[68:71], v[32:35], a[48:55], %[sidx]\n\t" \
    "v_smfmac_f32_16x16x64_bf16 v[72:75], v[32:35], a[56:63], %[sidx]\n\t" \
    "v_smfmac_f32_16x16x64_bf16 v[68:71], v[36:39], a[64:71], %[sidx]\n\t" \
    "v_smfmac_f32_16x16x64_bf16 v[72:75], v[36:39], a[72:79], %[sidx]\n\t" \
    "s_waitcnt lgkmcnt(0)\n\t" \
    "s_barrier\n\t" \
    "v_smfmac_f32_16x16x64_bf16 v[68:71], v[40:43], a[80:87], %[sidx]\n\t" \
    "ds_read_b64_tr_b16 v[28:29], %[l8] offset:24576\n\t" \
    "v_smfmac_f32_16x16x64_bf16 v[72:75], v[40:43], a[88:95], %[sidx]\n\t" \
    "ds_read_b64_tr_b16 v[30:31], %[l8] offset:25088\n\t" \
    "v_smfmac_f32_16x16x64_bf16 v[68:71], v[44:47], a[96:103], %[sidx]\n\t" \
    "ds_read_b64_tr_b16 v[32:33], %[l8] offset:25600\n\t" \
    "v_smfmac_f32_16x16x64_bf16 v[72:75], v[44:47], a[104:111], %[sidx]\n\t" \
    "ds_read_b64_tr_b16 v[34:35], %[l8] offset:26112\n\t" \
    "v_smfmac_f32_16x16x64_bf16 v[68:71], v[48:51], a[112:119], %[sidx]\n\t" \
    "ds_read_b64_tr_b16 v[36:37], %[l8] offset:26624\n\t" \
    "v_smfmac_f32_16x16x64_bf16 v[72:75], v[48:51], a[120:127], %[sidx]\n\t" \
    "ds_read_b64_tr_b16 v[38:39], %[l8] offset:27136\n\t" \
    "s_waitcnt lgkmcnt(4)\n\t" \
    "v_mfma_f32_16x16x32_bf16 v[52:55], v[76:79], v[28:31], 0\n\t" \
    "ds_read_b64_tr_b16 v[40:41], %[l8] offset:27648\n\t" \
    "v_mov_b32 v124, v125\n\t" \
    "v_mfma_f32_16x16x32_bf16 v[56:59], v[80:83], v[28:31], 0\n\t" \
    "ds_read_b64_tr_b16 v[42:43], %[l8] offset:28160\n\t" \
    "v_mov_b32 v125, v127\n\t" \
    "v_mfma_f32_16x16x32_bf16 v[60:63], v[28:31], v[28:31], 0\n\t" \
    "v_add_f32 v68, v68, v70\n\t" \
    "v_add_f32 v69, v69, v71\n\t" \
    "s_waitcnt lgkmcnt(4)\n\t" \
    "v_mfma_f32_16x16x32_bf16 v[52:55], v[84:87], v[32:35], v[52:55]\n\t" \
    "v_cvt_pk_bf16_f32 v126, v68, v69\n\t" \
    "v_add_f32 v72, v72, v74\n\t" \
    "v_mfma_f32_16x16x32_bf16 v[56:59], v[88:91], v[32:35], v[56:59]\n\t" \
    "v_add_f32 v73, v73, v75\n\t" \
    "v_cvt_pk_bf16_f32 v127, v72, v73\n\t" \
    "s_waitcnt lgkmcnt(2)\n\t" \
    "v_mfma_f32_16x16x32_bf16 v[52:55], v[92:95], v[36:39], v[52:55]\n\t" \
    "v_lshlrev_b32 v70, 2, %[l16]\n\t" \
    "v_mov_b32 v69, 0\n\t" \
    "v_mfma_f32_16x16x32_bf16 v[56:59], v[96:99], v[36:39], v[56:59]\n\t" \
    "v_mov_b64 v[44:45], 0\n\t" \
    "s_waitcnt lgkmcnt(0)\n\t" \
    "v_mfma_f32_16x16x32_bf16 v[52:55], v[100:103], v[40:43], v[52:55]\n\t" \
    "v_mov_b64 v[46:47], 0\n\t" \
    "v_mov_b64 v[48:49], 0\n\t" \
    "v_mfma_f32_16x16x32_bf16 v[56:59], v[104:107], v[40:43], v[56:59]\n\t" \
    "v_mov_b64 v[50:51], 0\n\t" \
    "v_mov_b64 v[100:101], 0\n\t" \
    "v_mfma_f32_16x16x32_bf16 v[60:63], v[32:35], v[32:35], v[60:63]\n\t" \
    "v_mov_b64 v[102:103], 0\n\t" \
    "v_mov_b64 v[104:105], 0\n\t" \
    "v_mfma_f32_16x16x32_bf16 v[60:63], v[36:39], v[36:39], v[60:63]\n\t" \
    "v_max3_f32 v64, v52, v53, v54\n\t" \
    "v_max3_f32 v64, v64, v55, v56\n\t" \
    "v_mfma_f32_16x16x32_bf16 v[60:63], v[40:43], v[40:43], v[60:63]\n\t" \
    "s_mov_b64 exec, s[98:99]\n\t" \
    "buffer_load_dwordx4 v[72:75], v70, %[rs], s38 offen sc1\n\t" \
    "s_mov_b64 exec, -1\n\t" \
    "v_max3_f32 v64, v64, v57, v58\n\t" \
    "v_max_f32 v64, v64, v59\n\t" \
    "v_mov_b64 v[106:107], 0\n\t" \
    "s_nop 1\n\t" \
    "v_max3_f32 v67, v64, v62, v63\n\t" \
    "v_max3_f32 v64, v64, v62, v63\n\t" \
    "s_nop 1\n\t" \
    "v_permlane16_swap_b32 v64, v67\n\t" \
    "v_max_f32 v65, v64, v67\n\t" \
    "v_max_f32 v64, v64, v67\n\t" \
    "s_nop 1\n\t" \
    "v_permlane32_swap_b32 v64, v65\n\t" \
    "v_max_f32 v66, v64, v65\n\t" \
    "v_pk_add_f32 v[62:63], v[62:63], v[66:67] op_sel_hi:[1,0] neg_lo:[0,1] neg_hi:[0,1]\n\t" \
    "v_pk_add_f32 v[52:53], v[52:53], v[66:67] op_sel_hi:[1,0] neg_lo:[0,1] neg_hi:[0,1]\n\t" \
    "v_pk_add_f32 v[54:55], v[54:55], v[66:67] op_sel_hi:[1,0] neg_lo:[0,1] neg_hi:[0,1]\n\t" \
    "v_pk_add_f32 v[56:57], v[56:57], v[66:67] op_sel_hi:[1,0] neg_lo:[0,1] neg_hi:[0,1]\n\t" \
    "v_pk_add_f32 v[58:59], v[58:59], v[66:67] op_sel_hi:[1,0] neg_lo:[0,1] neg_hi:[0,1]\n\t" \
    "v_exp_f32 v62, v62\n\t" \
    "v_exp_f32 v63, v63\n\t" \
    "v_exp_f32 v52, v52\n\t" \
    "v_cvt_pk_bf16_f32 v68, v62, v63\n\t" \
    "v_cvt_pk_bf16_f32 v19, v62, v63\n\t" \
    "v_exp_f32 v53, v53\n\t" \
    "v_mfma_f32_16x16x16_bf16 v[92:95], v[14:15], v[68:69], 0\n\t" \
    "v_exp_f32 v54, v54\n\t" \
    "v_mfma_f32_16x16x16_bf16 v[84:87], v[126:127], v[68:69], 0\n\t" \
    "v_exp_f32 v55, v55\n\t" \
    "v_mfma_f32_16x16x16_bf16 v[88:91], v[126:127], v[18:19], 0\n\t" \
    "v_exp_f32 v56, v56\n\t" \
    "v_exp_f32 v57, v57\n\t" \
    "v_cvt_pk_bf16_f32 v76, v52, v53\n\t" \
    "v_exp_f32 v58, v58\n\t" \
    "v_cvt_pk_bf16_f32 v77, v54, v55\n\t" \
    "v_exp_f32 v59, v59\n\t" \
    "v_cvt_pk_bf16_f32 v78, v56, v57\n\t" \
    "v_cvt_pk_bf16_f32 v79, v58, v59\n\t" \
    "s_nop 1\n\t" \
    "v_mfma_f32_16x16x32_bf16 v[92:95], v[14:17], v[76:79], v[92:95]\n\t" \
    "v_mfma_f32_16x16x32_bf16 v[84:87], v[20:23], v[76:79], v[84:87]\n\t" \
    "v_mfma_f32_16x16x32_bf16 v[88:91], v[24:27], v[76:79], v[88:91]\n\t" \
    "v_rcp_f32 v66, v92\n\t" \
    "s_nop 2\n\t" \
    "v_pk_mul_f32 v[84:85], v[84:85], v[66:67] op_sel_hi:[1,0]\n\t" \
    "v_pk_mul_f32 v[86:87], v[86:87], v[66:67] op_sel_hi:[1,0]\n\t" \
    "v_cvt_pk_bf16_f32 v96, v84, v85\n\t" \
    "v_cvt_pk_bf16_f32 v97, v86, v87\n\t" \
    "v_pk_mul_f32 v[88:89], v[88:89], v[66:67] op_sel_hi:[1,0]\n\t" \
    "v_pk_mul_f32 v[90:91], v[90:91], v[66:67] op_sel_hi:[1,0]\n\t" \
    "v_cvt_pk_bf16_f32 v98, v88, v89\n\t" \
    "v_cvt_pk_bf16_f32 v99, v90, v91\n\t" \
    "ds_write_b128 %[ow], v[96:99] offset:28672\n\t" \
    "buffer_store_dwordx4 v[124:127], %[l16], %[rs], s67 offen sc1\n\t" \
    "s_waitcnt vmcnt(6) lgkmcnt(0)\n\t" \
    "s_barrier\n\t" \
    "ds_read_b128 v[52:55], %[l16] offset:28672\n\t" \
    "ds_read_b128 v[56:59], %[l16] offset:29696\n\t" \
    "s_cmp_lg_u32 s98, 0\n\t" \
    "s_mov_b32 s74, 0\n\t" \
    "s_waitcnt lgkmcnt(1)\n\t" \
    "v_smfmac_f32_16x16x64_bf16 v[44:47], v[52:55], a[128:135], %[sidx]\n\t" \
    "v_smfmac_f32_16x16x64_bf16 v[48:51], v[52:55], a[136:143], %[sidx]\n\t" \
    "s_waitcnt lgkmcnt(0)\n\t" \
    "v_smfmac_f32_16x16x64_bf16 v[44:47], v[56:59], a[160:167], %[sidx]\n\t" \
    "ds_read_b128 v[92:95], %[l16] offset:30720\n\t" \
    "v_smfmac_f32_16x16x64_bf16 v[48:51], v[56:59], a[168:175], %[sidx]\n\t" \
    "v_smfmac_f32_16x16x64_bf16 v[100:103], v[52:55], a[144:151], %[sidx]\n\t" \
    "v_add_f32 v44, v44, v46\n\t" \
    "v_smfmac_f32_16x16x64_bf16 v[100:103], v[56:59], a[176:183], %[sidx]\n\t" \
    "v_add_f32 v45, v45, v47\n\t" \
    "v_smfmac_f32_16x16x64_bf16 v[104:107], v[52:55], a[152:159], %[sidx]\n\t" \
    "v_cvt_pk_bf16_f32 v60, v44, v45\n\t" \
    "v_smfmac_f32_16x16x64_bf16 v[104:107], v[56:59], a[184:191], %[sidx]\n\t" \
    "v_add_f32 v48, v48, v50\n\t" \
    "v_add_f32 v49, v49, v51\n\t" \
    "v_cvt_pk_bf16_f32 v61, v48, v49\n\t" \
    "v_add_f32 v100, v100, v102\n\t" \
    "v_add_f32 v101, v101, v103\n\t" \
    "v_cvt_pk_bf16_f32 v62, v100, v101\n\t" \
    "s_nop 1\n\t" \
    "v_pk_add_f32 v[104:105], v[104:105], v[106:107]\n\t" \
    "v_cvt_pk_bf16_f32 v63, v104, v105\n\t" \
    "buffer_store_dwordx4 v[60:63], %[pst], %[rs], s61 offen sc1\n\t" \
    "s_cbranch_scc0 LKV_%=\n\t" \
    "LCP_%=:\n\t" \
    "s_waitcnt vmcnt(2)\n\t" \
    "v_xor_b32 v69, s39, v72\n\t" \
    "s_memtime s[84:85]\n\t" \
    "v_xor_b32 v71, s39, v73\n\t" \
    "v_or_b32 v69, v69, v71\n\t" \
    "v_xor_b32 v71, s39, v74\n\t" \
    "v_xor_b32 v68, s39, v75\n\t" \
    "v_or3_b32 v69, v69, v71, v68\n\t" \
    "v_subrev_u32 v72, s39, v72\n\t" \
    "v_subrev_u32 v73, s39, v73\n\t" \
    "v_subrev_u32 v74, s39, v74\n\t" \
    "v_subrev_u32 v75, s39, v75\n\t" \
    "v_lshlrev_b32 v72, 8, v72\n\t" \
    "v_lshlrev_b32 v73, 8, v73\n\t" \
    "v_lshlrev_b32 v74, 8, v74\n\t" \
    "v_lshlrev_b32 v75, 8, v75\n\t" \
    "v_max3_i32 v68, v72, v73, v74\n\t" \
    "v_max_i32 v68, v68, v75\n\t" \
    "v_cmp_lt_u32 vcc, 0x3ffffff, v69\n\t" \
    "s_cbranch_vccnz LCS_%=\n\t" \
    "v_max_i32_dpp v68, v68, v68 row_shr:1 row_mask:0xf bank_mask:0xf\n\t" \
    "s_nop 1\n\t" \
    "v_max_i32_dpp v68, v68, v68 row_shr:2 row_mask:0xf bank_mask:0xf\n\t" \
    "s_nop 1\n\t" \
    "v_max_i32_dpp v68, v68, v68 row_shr:4 row_mask:0xf bank_mask:0xf\n\t" \
    "s_nop 1\n\t" \
    "v_max_i32_dpp v68, v68, v68 row_shr:8 row_mask:0xf bank_mask:0xf\n\t" \
    "s_nop 1\n\t" \
    "v_max_i32_dpp v68, v68, v68 row_bcast:15 row_mask:0xa bank_mask:0xf\n\t" \
    "s_nop 1\n\t" \
    "v_max_i32_dpp v68, v68, v68 row_bcast:31 row_mask:0xc bank_mask:0xf\n\t" \
    "s_nop 1\n\t" \
    "v_readlane_b32 s77, v68, 63\n\t" \
    "s_waitcnt lgkmcnt(0)\n\t" \
    "s_ashr_i32 s77, s77, 8\n\t" \
    "s_add_u32 s77, s57, s77\n\t" \
    "s_add_u32 s58, s77, s87\n\t" \
    "s_add_u32 s77, s77, s86\n\t" \
    "s_getpc_b64 s[54:55]\n\t" \
    "LP0_%=:\n\t" \
    "s_sub_u32 s75, s77, s84\n\t" \
    "s_max_i32 s75, s75, 0\n\t" \
    "s_min_i32 s75, s75, 1023\n\t" \
    "s_lshr_b32 s75, s75, 2\n\t" \
    "s_and_b32 s75, s75, 0xfc\n\t" \
    "s_sub_u32 s75, LE0_%=-LP0_%=, s75\n\t" \
    "s_add_u32 s54, s54, s75\n\t" \
    "s_addc_u32 s55, s55, 0\n\t" \
    "s_setpc_b64 s[54:55]\n\t" \
    "s_nop 3\n\t" \
    "s_nop 3\n\t" \
    "s_nop 3\n\t" \
    "s_nop 3\n\t" \
    "s_nop 3\n\t" \
    "s_nop 3\n\t" \
    "s_nop 3\n\t" \
    "s_nop 3\n\t" \
    "s_nop 3\n\t" \
    "s_nop 3\n\t" \
    "s_nop 3\n\t" \
    "s_nop 3\n\t" \
    "s_nop 3\n\t" \
    "s_nop 3\n\t" \
    "s_nop 3\n\t" \
    "s_nop 3\n\t" \
    "s_nop 3\n\t" \
    "s_nop 3\n\t" \
    "s_nop 3\n\t" \
    "s_nop 3\n\t" \
    "s_nop 3\n\t" \
    "s_nop 3\n\t" \
    "s_nop 3\n\t" \
    "s_nop 3\n\t" \
    "s_nop 3\n\t" \
    "s_nop 3\n\t" \
    "s_nop 3\n\t" \
    "s_nop 3\n\t" \
    "s_nop 3\n\t" \
    "s_nop 3\n\t" \
    "s_nop 3\n\t" \
    "s_nop 3\n\t" \
    "s_nop 3\n\t" \
    "s_nop 3\n\t" \
    "s_nop 3\n\t" \
    "s_nop 3\n\t" \
    "s_nop 3\n\t" \
    "s_nop 3\n\t" \
    "s_nop 3\n\t" \
    "s_nop 3\n\t" \
    "s_nop 3\n\t" \
    "s_nop 3\n\t" \
    "s_nop 3\n\t" \
    "s_nop 3\n\t" \
    "s_nop 3\n\t" \
    "s_nop 3\n\t" \
    "s_nop 3\n\t" \
    "s_nop 3\n\t" \
    "s_nop 3\n\t" \
    "s_nop 3\n\t" \
    "s_nop 3\n\t" \
    "s_nop 3\n\t" \
    "s_nop 3\n\t" \
    "s_nop 3\n\t" \
    "s_nop 3\n\t" \
    "s_nop 3\n\t" \
    "s_nop 3\n\t" \
    "s_nop 3\n\t" \
    "s_nop 3\n\t" \
    "s_nop 3\n\t" \
    "s_nop 3\n\t" \
    "s_nop 3\n\t" \
    "s_nop 3\n\t" \
    "s_nop 3\n\t" \
    "LE0_%=:\n\t" \
    "s_mov_b32 s74, 0\n\t" \
    "buffer_load_dwordx4 v[76:79], %[perm], %[rs], s63 offen sc1\n\t" \
    "buffer_load_dwordx4 v[80:83], %[perm], %[rs], s63 offen offset:1024 sc1\n\t" \
    "buffer_load_dwordx4 v[84:87], %[perm], %[rs], s63 offen offset:2048 sc1\n\t" \
    "buffer_load_dwordx4 v[88:91], %[perm], %[rs], s63 offen offset:3072 sc1\n\t" \
    "v_cmp_lt_u32 vcc, 0x1ffffff, v69\n\t" \
    "v_and_b32 v69, 0x1000000, v69\n\t" \
    "v_cmp_ne_u32 s[82:83], 0, v69\n\t" \
    "s_cmp_lg_u64 vcc, 0\n\t" \
    "s_cselect_b32 s75, 24, -4\n\t" \
    "s_cselect_b64 s[82:83], 0, s[82:83]\n\t" \
    "s_add_u32 s86, s86, s75\n\t" \
    "s_add_u32 s87, s87, s75\n\t" \
    "s_cmp_lg_u64 s[82:83], 0\n\t" \
    "s_cselect_b32 s75, 24, -4\n\t" \
    "s_add_u32 s87, s87, s75\n\t" \
    "s_memtime s[84:85]\n\t" \
    "s_memrealtime s[40:41]\n\t" \
    "s_nop 2\n\t" \
    "s_memrealtime s[42:43]\n\t" \
    "s_nop 2\n\t" \
    "s_memrealtime s[44:45]\n\t" \
    "s_nop 2\n\t" \
    "s_memrealtime s[46:47]\n\t" \
    "s_nop 2\n\t" \
    "s_memrealtime s[48:49]\n\t" \
    "s_nop 2\n\t" \
    "s_memrealtime s[50:51]\n\t" \
    "s_nop 2\n\t" \
    "s_memrealtime s[52:53]\n\t" \
    "s_nop 2\n\t" \
    "s_waitcnt lgkmcnt(0)\n\t" \
    "s_mov_b32 s75, 0\n\t" \
    "s_sub_u32 s43, s42, s40\n\t" \
    "s_mul_i32 s43, s43, 23\n\t" \
    "s_sub_u32 s43, s43, 17\n\t" \
    "s_max_i32 s75, s75, s43\n\t" \
    "s_sub_u32 s45, s44, s40\n\t" \
    "s_mul_i32 s45, s45, 23\n\t" \
    "s_sub_u32 s45, s45, 34\n\t" \
    "s_max_i32 s75, s75, s45\n\t" \
    "s_sub_u32 s47, s46, s40\n\t" \
    "s_mul_i32 s47, s47, 23\n\t" \
    "s_sub_u32 s47, s47, 51\n\t" \
    "s_max_i32 s75, s75, s47\n\t" \
    "s_sub_u32 s49, s48, s40\n\t" \
    "s_mul_i32 s49, s49, 23\n\t" \
    "s_sub_u32 s49, s49, 68\n\t" \
    "s_max_i32 s75, s75, s49\n\t" \
    "s_sub_u32 s51, s50, s40\n\t" \
    "s_mul_i32 s51, s51, 23\n\t" \
    "s_sub_u32 s51, s51, 85\n\t" \
    "s_max_i32 s75, s75, s51\n\t" \
    "s_sub_u32 s53, s52, s40\n\t" \
    "s_mul_i32 s53, s53, 23\n\t" \
    "s_sub_u32 s53, s53, 102\n\t" \
    "s_max_i32 s75, s75, s53\n\t" \
    "s_mul_i32 s40, s40, 187\n\t" \
    "s_lshr_b32 s40, s40, 3\n\t" \
    "s_add_u32 s56, s40, s75\n\t" \
    "s_sub_u32 s56, s56, s84\n\t" \
    "s_nop 0\n\t" \
    "buffer_load_dwordx4 v[28:31], %[perm], %[rs], s63 offen sc1\n\t" \
    "buffer_load_dwordx4 v[32:35], %[perm], %[rs], s63 offen offset:1024 sc1\n\t" \
    "buffer_load_dwordx4 v[36:39], %[perm], %[rs], s63 offen offset:2048 sc1\n\t" \
    "buffer_load_dwordx4 v[40:43], %[perm], %[rs], s63 offen offset:3072 sc1\n\t" \
    "s_add_u32 s66, s65, %[smld]\n\t" \
    "LRP_%=:\n\t" \
    "s_waitcnt vmcnt(7)\n\t" \
    "v_mfma_f32_16x16x32_bf16 v[96:99], v[76:79], v[92:95], 0\n\t" \
    "v_max3_u32 v46, v76, v77, v78\n\t" \
    "v_max_u32 v46, v46, v79\n\t" \
    "s_waitcnt vmcnt(6)\n\t" \
    "v_mfma_f32_16x16x32_bf16 v[96:99], v[80:83], v[92:95], v[96:99]\n\t" \
    "v_max3_u32 v46, v80, v81, v46\n\t" \
    "v_max3_u32 v46, v82, v83, v46\n\t" \
    "s_waitcnt vmcnt(5)\n\t" \
    "v_mfma_f32_16x16x32_bf16 v[96:99], v[84:87], v[92:95], v[96:99]\n\t" \
    "v_max3_u32 v46, v84, v85, v46\n\t" \
    "v_max3_u32 v46, v86, v87, v46\n\t" \
    "s_waitcnt vmcnt(4)\n\t" \
    "v_mfma_f32_16x16x32_bf16 v[96:99], v[88:91], v[92:95], v[96:99]\n\t" \
    "v_max3_u32 v46, v88, v89, v46\n\t" \
    "v_max3_u32 v46, v90, v91, v46\n\t" \
    "v_cmp_le_u32 vcc, 0xff810000, v46\n\t" \
    "s_cbranch_vccnz LR2_%=\n\t" \
    "LRC_%=:\n\t" \
    "s_memtime s[84:85]\n\t" \
    "s_nop 2\n\t" \
    "v_pk_add_f32 v[96:97], v[96:97], v[98:99]\n\t" \
    "v_cvt_pk_bf16_f32 v96, v96, v97\n\t" \
    "buffer_store_dword v96, %[mst], %[rs], s65 offen sc1\n\t" \
    "s_waitcnt lgkmcnt(0)\n\t" \
    "s_getpc_b64 s[54:55]\n\t" \
    "LP1_%=:\n\t" \
    "s_sub_u32 s75, s58, s84\n\t" \
    "s_max_i32 s75, s75, 120\n\t" \
    "s_min_i32 s75, s75, 1023\n\t" \
    "s_lshr_b32 s75, s75, 2\n\t" \
    "s_and_b32 s75, s75, 0xfc\n\t" \
    "s_sub_u32 s75, LE1_%=-LP1_%=, s75\n\t" \
    "s_add_u32 s54, s54, s75\n\t" \
    "s_addc_u32 s55, s55, 0\n\t" \
    "s_setpc_b64 s[54:55]\n\t" \
    "s_nop 3\n\t" \
    "s_nop 3\n\t" \
    "s_nop 3\n\t" \
    "s_nop 3\n\t" \
    "s_nop 3\n\t" \
    "s_nop 3\n\t" \
    "s_nop 3\n\t" \
    "s_nop 3\n\t" \
    "s_nop 3\n\t" \
    "s_nop 3\n\t" \
    "s_nop 3\n\t" \
    "s_nop 3\n\t" \
    "s_nop 3\n\t" \
    "s_nop 3\n\t" \
    "s_nop 3\n\t" \
    "s_nop 3\n\t" \
    "s_nop 3\n\t" \
    "s_nop 3\n\t" \
    "s_nop 3\n\t" \
    "s_nop 3\n\t" \
    "s_nop 3\n\t" \
    "s_nop 3\n\t" \
    "s_nop 3\n\t" \
    "s_nop 3\n\t" \
    "s_nop 3\n\t" \
    "s_nop 3\n\t" \
    "s_nop 3\n\t" \
    "s_nop 3\n\t" \
    "s_nop 3\n\t" \
    "s_nop 3\n\t" \
    "s_nop 3\n\t" \
    "s_nop 3\n\t" \
    "s_nop 3\n\t" \
    "s_nop 3\n\t" \
    "s_nop 3\n\t" \
    "s_nop 3\n\t" \
    "s_nop 3\n\t" \
    "s_nop 3\n\t" \
    "s_nop 3\n\t" \
    "s_nop 3\n\t" \
    "s_nop 3\n\t" \
    "s_nop 3\n\t" \
    "s_nop 3\n\t" \
    "s_nop 3\n\t" \
    "s_nop 3\n\t" \
    "s_nop 3\n\t" \
    "s_nop 3\n\t" \
    "s_nop 3\n\t" \
    "s_nop 3\n\t" \
    "s_nop 3\n\t" \
    "s_nop 3\n\t" \
    "s_nop 3\n\t" \
    "s_nop 3\n\t" \
    "s_nop 3\n\t" \
    "s_nop 3\n\t" \
    "s_nop 3\n\t" \
    "s_nop 3\n\t" \
    "s_nop 3\n\t" \
    "s_nop 3\n\t" \
    "s_nop 3\n\t" \
    "s_nop 3\n\t" \
    "s_nop 3\n\t" \
    "s_nop 3\n\t" \
    "s_nop 3\n\t" \
    "LE1_%=:\n\t" \
    "s_waitcnt vmcnt(1)\n\t" \
    "s_barrier\n\t" \
    "buffer_load_dwordx4 v[20:23], %[l16], %[rs], s66 offen sc1\n\t" \
    "buffer_load_dwordx4 v[24:27], %[l16], %[rs], s66 offen offset:1024 sc1\n\t" \
    "s_lshl_b32 s75, %[wv], 12\n\t" \
    "v_add_u32 v45, s75, %[l16]\n\t" \
    "ds_write_b128 v45, v[108:111] offset:8192\n\t" \
    "ds_write_b128 v45, v[112:115] offset:9216\n\t" \
    "ds_write_b128 v45, v[116:119] offset:10240\n\t" \
    "ds_write_b128 v45, v[120:123] offset:11264\n\t" \
    "s_mov_b32 s74, 0\n\t" \
    "s_add_u32 s60, s60, 1\n\t" \
    "s_mov_b32 s81, 0\n\t" \
    "ds_read_b128 v[76:79], %[wob]\n\t" \
    "ds_read_b128 v[80:83], %[wob] offset:1024\n\t" \
    "ds_read_b128 v[84:87], %[wob] offset:2048\n\t" \
    "ds_read_b128 v[88:91], %[wob] offset:3072\n\t" \
    "ds_read_b128 v[92:95], %[wob] offset:4096\n\t" \
    "ds_read_b128 v[96:99], %[wob] offset:5120\n\t" \
    "ds_read_b128 v[100:103], %[wob] offset:6144\n\t" \
    "ds_read_b128 v[104:107], %[wob] offset:7168\n\t" \
    "v_mov_b64 v[52:53], 0\n\t" \
    "v_mov_b64 v[54:55], 0\n\t" \
    "v_mov_b64 v[56:57], 0\n\t" \
    "v_mov_b64 v[58:59], 0\n\t" \
    "v_mov_b64 v[60:61], 0\n\t" \
    "v_mov_b64 v[62:63], 0\n\t" \
    "v_mov_b64 v[64:65], 0\n\t" \
    "v_mov_b64 v[66:67], 0\n\t" \
    "v_mov_b64 v[68:69], 0\n\t" \
    "v_mov_b64 v[70:71], 0\n\t" \
    "v_mov_b64 v[72:73], 0\n\t" \
    "v_mov_b64 v[74:75], 0\n\t" \
    "v_mov_b32 v124, 0xff81ff81\n\t" \
    "v_mov_b32 v125, 0xff81ff81\n\t" \
    "v_mov_b32 v126, 0xff81ff81\n\t" \
    "v_mov_b32 v127, 0xff81ff81\n\t" \
    "s_xor_b32 s64, s64, 0x80000\n\t" \
    "s_xor_b32 s61, s61, 0x80000\n\t" \
    "s_xor_b32 s63, s63, 0x80000\n\t" \
    "s_add_u32 s65, s65, 0x4000\n\t" \
    "s_add_u32 s67, s67, 4096\n\t" \
    "s_add_u32 s69, s69, 0x40000\n\t" \
    "s_add_u32 s72, s72, 0x1000\n\t" \
    "s_waitcnt lgkmcnt(8)\n\t" \
    "v_mov_b32 v120, s60\n\t" \
    "v_mov_b32 v121, s80\n\t" \
    "s_and_b32 s75, s60, 1\n\t" \
    "s_lshl_b32 s75, s75, 12\n\t" \
    "s_add_u32 s38, s75, s68\n\t" \
    "s_add_u32 s59, s38, %[sca]\n\t" \
    "s_lshl_b32 s75, s60, 26\n\t" \
    "s_or_b32 s62, s62, s75\n\t" \
    "s_nop 9\n\t" \
    "buffer_load_dwordx4 v[108:111], %[l16], %[rs], s66 offen sc1\n\t" \
    "buffer_load_dwordx4 v[112:115], %[l16], %[rs], s66 offen offset:1024 sc1\n\t" \
    "s_cmp_lt_u32 s60, 63\n\t" \
    "s_cbranch_scc1 LTOP_%=\n\t" \
    "LFIN_%=:\n\t" \
    "s_waitcnt vmcnt(3)\n\t" \
    "v_max3_u32 v116, v20, v21, v22\n\t" \
    "s_waitcnt vmcnt(2)\n\t" \
    "v_max3_u32 v116, v23, v24, v116\n\t" \
    "v_max3_u32 v116, v25, v26, v116\n\t" \
    "v_max_u32 v116, v27, v116\n\t" \
    "v_cmp_le_u32 vcc, 0xff810000, v116\n\t" \
    "s_cbranch_vccnz LG1_%=\n\t" \
    "LLAST_%=:\n\t" \
    "s_waitcnt lgkmcnt(0)\n\t" \
    "ds_write_b128 %[mw], v[20:23]\n\t" \
    "ds_write_b128 %[mw], v[24:27] offset:1024\n\t" \
    "s_mov_b64 exec, 1\n\t" \
    "buffer_store_dwordx2 v[120:121], off, %[rs], s72 sc1\n\t" \
    "s_mov_b64 exec, -1\n\t" \
    "v_smfmac_f32_16x16x64_bf16 v[60:63], v[20:23], v[192:199], %[sidx]\n\t" \
    "v_smfmac_f32_16x16x64_bf16 v[64:67], v[20:23], a[192:199], %[sidx]\n\t" \
    "v_smfmac_f32_16x16x64_bf16 v[60:63], v[24:27], v[200:207], %[sidx]\n\t" \
    "v_smfmac_f32_16x16x64_bf16 v[64:67], v[24:27], a[200:207], %[sidx]\n\t" \
    "s_waitcnt lgkmcnt(0)\n\t" \
    "s_barrier\n\t" \
    "v_smfmac_f32_16x16x64_bf16 v[68:71], v[20:23], a[0:7], %[sidx]\n\t" \
    "ds_read_b128 v[28:31], %[mr1]\n\t" \
    "ds_read_b128 v[32:35], %[mr1] offset:1024\n\t" \
    "v_smfmac_f32_16x16x64_bf16 v[72:75], v[20:23], a[8:15], %[sidx]\n\t" \
    "ds_read_b128 v[36:39], %[mr2]\n\t" \
    "ds_read_b128 v[40:43], %[mr2] offset:1024\n\t" \
    "v_smfmac_f32_16x16x64_bf16 v[68:71], v[24:27], a[16:23], %[sidx]\n\t" \
    "ds_read_b128 v[44:47], %[mr3]\n\t" \
    "v_smfmac_f32_16x16x64_bf16 v[72:75], v[24:27], a[24:31], %[sidx]\n\t" \
    "ds_read_b128 v[48:51], %[mr3] offset:1024\n\t" \
    "s_waitcnt lgkmcnt(5)\n\t" \
    "v_smfmac_f32_16x16x64_bf16 v[60:63], v[28:31], v[208:215], %[sidx]\n\t" \
    "v_smfmac_f32_16x16x64_bf16 v[64:67], v[28:31], a[208:215], %[sidx]\n\t" \
    "s_waitcnt lgkmcnt(4)\n\t" \
    "v_smfmac_f32_16x16x64_bf16 v[60:63], v[32:35], v[216:223], %[sidx]\n\t" \
    "v_smfmac_f32_16x16x64_bf16 v[64:67], v[32:35], a[216:223], %[sidx]\n\t" \
    "s_waitcnt lgkmcnt(3)\n\t" \
    "v_smfmac_f32_16x16x64_bf16 v[60:63], v[36:39], v[224:231], %[sidx]\n\t" \
    "v_smfmac_f32_16x16x64_bf16 v[64:67], v[36:39], a[224:231], %[sidx]\n\t" \
    "s_waitcnt lgkmcnt(2)\n\t" \
    "v_smfmac_f32_16x16x64_bf16 v[60:63], v[40:43], v[232:239], %[sidx]\n\t" \
    "v_smfmac_f32_16x16x64_bf16 v[64:67], v[40:43], a[232:239], %[sidx]\n\t" \
    "s_waitcnt lgkmcnt(1)\n\t" \
    "v_smfmac_f32_16x16x64_bf16 v[60:63], v[44:47], v[240:247], %[sidx]\n\t" \
    "v_smfmac_f32_16x16x64_bf16 v[64:67], v[44:47], a[240:247], %[sidx]\n\t" \
    "s_waitcnt lgkmcnt(0)\n\t" \
    "v_smfmac_f32_16x16x64_bf16 v[60:63], v[48:51], v[248:255], %[sidx]\n\t" \
    "v_smfmac_f32_16x16x64_bf16 v[64:67], v[48:51], a[248:255], %[sidx]\n\t" \
    "v_smfmac_f32_16x16x64_bf16 v[68:71], v[28:31], a[32:39], %[sidx]\n\t" \
    "v_smfmac_f32_16x16x64_bf16 v[72:75], v[28:31], a[40:47], %[sidx]\n\t" \
    "v_smfmac_f32_16x16x64_bf16 v[68:71], v[32:35], a[48:55], %[sidx]\n\t" \
    "v_add_f32 v60, v60, v62\n\t" \
    "v_smfmac_f32_16x16x64_bf16 v[72:75], v[32:35], a[56:63], %[sidx]\n\t" \
    "v_add_f32 v61, v61, v63\n\t" \
    "v_smfmac_f32_16x16x64_bf16 v[68:71], v[36:39], a[64:71], %[sidx]\n\t" \
    "v_cvt_pk_bf16_f32 v124, v60, v61\n\t" \
    "v_smfmac_f32_16x16x64_bf16 v[72:75], v[36:39], a[72:79], %[sidx]\n\t" \
    "v_add_f32 v64, v64, v66\n\t" \
    "v_smfmac_f32_16x16x64_bf16 v[68:71], v[40:43], a[80:87], %[sidx]\n\t" \
    "v_add_f32 v65, v65, v67\n\t" \
    "v_smfmac_f32_16x16x64_bf16 v[72:75], v[40:43], a[88:95], %[sidx]\n\t" \
    "v_cvt_pk_bf16_f32 v125, v64, v65\n\t" \
    "v_smfmac_f32_16x16x64_bf16 v[68:71], v[44:47], a[96:103], %[sidx]\n\t" \
    "v_smfmac_f32_16x16x64_bf16 v[72:75], v[44:47], a[104:111], %[sidx]\n\t" \
    "v_smfmac_f32_16x16x64_bf16 v[68:71], v[48:51], a[112:119], %[sidx]\n\t" \
    "v_smfmac_f32_16x16x64_bf16 v[72:75], v[48:51], a[120:127], %[sidx]\n\t" \
    "s_nop 3\n\t" \
    "v_add_f32 v68, v68, v70\n\t" \
    "v_add_f32 v69, v69, v71\n\t" \
    "v_cvt_pk_bf16_f32 v126, v68, v69\n\t" \
    "s_nop 0\n\t" \
    "v_add_f32 v72, v72, v74\n\t" \
    "v_add_f32 v73, v73, v75\n\t" \
    "v_cvt_pk_bf16_f32 v127, v72, v73\n\t" \
    "buffer_store_dwordx4 v[124:127], %[l16], %[rs], s67 offen sc1\n\t" \
    "s_add_u32 s72, s72, 0x1000\n\t" \
    "v_mov_b32 v120, 64\n\t" \
    "s_waitcnt vmcnt(0) lgkmcnt(0)\n\t" \
    "s_barrier\n\t" \
    "s_mov_b64 exec, 1\n\t" \
    "buffer_store_dwordx2 v[120:121], off, %[rs], s72 sc1\n\t" \
    "s_mov_b64 exec, -1\n\t" \
    "s_waitcnt vmcnt(0)\n\t" \
    "s_branch LEND_%=\n\t" \
    "LKV_%=:\n\t" \
    "s_add_u32 s66, s65, %[smld]\n\t" \
    "s_lshl_b32 s75, %[wv], 12\n\t" \
    "v_add_u32 v45, s75, %[l16]\n\t" \
    "s_waitcnt vmcnt(6)\n\t" \
    "ds_write_b128 v45, v[108:111] offset:8192\n\t" \
    "s_waitcnt vmcnt(4)\n\t" \
    "ds_write_b128 v45, v[112:115] offset:9216\n\t" \
    "s_waitcnt vmcnt(3)\n\t" \
    "ds_write_b128 v45, v[116:119] offset:10240\n\t" \
    "s_waitcnt vmcnt(2)\n\t" \
    "ds_write_b128 v45, v[120:123] offset:11264\n\t" \
    "s_mov_b32 s74, 0\n\t" \
    "s_add_u32 s60, s60, 1\n\t" \
    "s_mov_b32 s81, 0\n\t" \
    "ds_read_b128 v[76:79], %[wob]\n\t" \
    "ds_read_b128 v[80:83], %[wob] offset:1024\n\t" \
    "ds_read_b128 v[84:87], %[wob] offset:2048\n\t" \
    "ds_read_b128 v[88:91], %[wob] offset:3072\n\t" \
    "ds_read_b128 v[92:95], %[wob] offset:4096\n\t" \
    "ds_read_b128 v[96:99], %[wob] offset:5120\n\t" \
    "ds_read_b128 v[100:103], %[wob] offset:6144\n\t" \
    "ds_read_b128 v[104:107], %[wob] offset:7168\n\t" \
    "v_mov_b64 v[52:53], 0\n\t" \
    "v_mov_b64 v[54:55], 0\n\t" \
    "v_mov_b64 v[56:57], 0\n\t" \
    "v_mov_b64 v[58:59], 0\n\t" \
    "v_mov_b64 v[60:61], 0\n\t" \
    "v_mov_b64 v[62:63], 0\n\t" \
    "v_mov_b64 v[64:65], 0\n\t" \
    "v_mov_b64 v[66:67], 0\n\t" \
    "v_mov_b64 v[68:69], 0\n\t" \
    "v_mov_b64 v[70:71], 0\n\t" \
    "v_mov_b64 v[72:73], 0\n\t" \
    "v_mov_b64 v[74:75], 0\n\t" \
    "v_mov_b32 v124, 0xff81ff81\n\t" \
    "v_mov_b32 v125, 0xff81ff81\n\t" \
    "v_mov_b32 v126, 0xff81ff81\n\t" \
    "v_mov_b32 v127, 0xff81ff81\n\t" \
    "s_xor_b32 s64, s64, 0x80000\n\t" \
    "s_xor_b32 s61, s61, 0x80000\n\t" \
    "s_xor_b32 s63, s63, 0x80000\n\t" \
    "s_add_u32 s65, s65, 0x4000\n\t" \
    "s_add_u32 s67, s67, 4096\n\t" \
    "s_add_u32 s69, s69, 0x40000\n\t" \
    "s_add_u32 s72, s72, 0x1000\n\t" \
    "s_waitcnt lgkmcnt(8)\n\t" \
    "v_mov_b32 v120, s60\n\t" \
    "v_mov_b32 v121, s80\n\t" \
    "s_and_b32 s75, s60, 1\n\t" \
    "s_lshl_b32 s75, s75, 12\n\t" \
    "s_add_u32 s38, s75, s68\n\t" \
    "s_add_u32 s59, s38, %[sca]\n\t" \
    "s_lshl_b32 s75, s60, 26\n\t" \
    "s_or_b32 s62, s62, s75\n\t" \
    "s_waitcnt lgkmcnt(0)\n\t" \
    "s_memtime s[84:85]\n\t" \
    "s_memrealtime s[40:41]\n\t" \
    "s_nop 2\n\t" \
    "s_memrealtime s[42:43]\n\t" \
    "s_nop 2\n\t" \
    "s_memrealtime s[44:45]\n\t" \
    "s_nop 2\n\t" \
    "s_memrealtime s[46:47]\n\t" \
    "s_nop 2\n\t" \
    "s_memrealtime s[48:49]\n\t" \
    "s_nop 2\n\t" \
    "s_memrealtime s[50:51]\n\t" \
    "s_nop 2\n\t" \
    "s_memrealtime s[52:53]\n\t" \
    "s_nop 2\n\t" \
    "s_waitcnt lgkmcnt(0)\n\t" \
    "s_mov_b32 s75, 0\n\t" \
    "s_sub_u32 s43, s42, s40\n\t" \
    "s_mul_i32 s43, s43, 23\n\t" \
    "s_sub_u32 s43, s43, 17\n\t" \
    "s_max_i32 s75, s75, s43\n\t" \
    "s_sub_u32 s45, s44, s40\n\t" \
    "s_mul_i32 s45, s45, 23\n\t" \
    "s_sub_u32 s45, s45, 34\n\t" \
    "s_max_i32 s75, s75, s45\n\t" \
    "s_sub_u32 s47, s46, s40\n\t" \
    "s_mul_i32 s47, s47, 23\n\t" \
    "s_sub_u32 s47, s47, 51\n\t" \
    "s_max_i32 s75, s75, s47\n\t" \
    "s_sub_u32 s49, s48, s40\n\t" \
    "s_mul_i32 s49, s49, 23\n\t" \
    "s_sub_u32 s49, s49, 68\n\t" \
    "s_max_i32 s75, s75, s49\n\t" \
    "s_sub_u32 s51, s50, s40\n\t" \
    "s_mul_i32 s51, s51, 23\n\t" \
    "s_sub_u32 s51, s51, 85\n\t" \
    "s_max_i32 s75, s75, s51\n\t" \
    "s_sub_u32 s53, s52, s40\n\t" \
    "s_mul_i32 s53, s53, 23\n\t" \
    "s_sub_u32 s53, s53, 102\n\t" \
    "s_max_i32 s75, s75, s53\n\t" \
    "s_mul_i32 s40, s40, 187\n\t" \
    "s_lshr_b32 s40, s40, 3\n\t" \
    "s_add_u32 s56, s40, s75\n\t" \
    "s_sub_u32 s56, s56, s84\n\t" \
    "s_add_u32 s73, s60, 2\n\t" \
    "s_min_u32 s73, s73, 64\n\t" \
    "s_cmp_ge_u32 s71, s73\n\t" \
    "s_cbranch_scc1 LKVOK_%=\n\t" \
    "s_cmp_le_u32 s73, 4\n\t" \
    "s_cselect_b32 s70, 128, 0\n\t" \
    "s_add_u32 s70, s70, %[sflg]\n\t" \
    "LKVP_%=:\n\t" \
    "buffer_load_dwordx2 v[44:45], off, %[rs], s70 sc1\n\t" \
    "s_waitcnt vmcnt(0)\n\t" \
    "v_readfirstlane_b32 s71, v44\n\t" \
    "v_readfirstlane_b32 s75, v45\n\t" \
    "s_nop 3\n\t" \
    "s_cmp_eq_u32 s75, s80\n\t" \
    "s_cselect_b32 s71, s71, 0\n\t" \
    "s_cmp_le_u32 s71, 64\n\t" \
    "s_cselect_b32 s71, s71, 0\n\t" \
    "s_cmp_ge_u32 s71, s73\n\t" \
    "s_cbranch_scc0 LKVS_%=\n\t" \
    "LKVOK_%=:\n\t" \
    "s_waitcnt vmcnt(0) lgkmcnt(0)\n\t" \
    "s_mov_b32 s74, 0\n\t" \
    "s_barrier\n\t" \
    "buffer_load_dwordx4 v[20:23], %[l16], %[rs], s66 offen sc1\n\t" \
    "buffer_load_dwordx4 v[24:27], %[l16], %[rs], s66 offen offset:1024 sc1\n\t" \
    "s_nop 15\n\t" \
    "s_nop 15\n\t" \
    "s_nop 15\n\t" \
    "s_nop 15\n\t" \
    "s_nop 15\n\t" \
    "s_nop 4\n\t" \
    "buffer_load_dwordx4 v[108:111], %[l16], %[rs], s66 offen sc1\n\t" \
    "buffer_load_dwordx4 v[112:115], %[l16], %[rs], s66 offen offset:1024 sc1\n\t" \
    "s_cmp_lt_u32 s60, 63\n\t" \
    "s_cbranch_scc1 LTOP_%=\n\t" \
    "s_branch LFIN_%=\n\t" \
    "LKVS_%=:\n\t" \
    "s_add_u32 s74, s74, 1\n\t" \
    "s_and_b32 s75, s74, 63\n\t" \
    "s_cbranch_scc1 LKVP_%=\n\t" \
    "buffer_load_dword v44, off, %[rs], s76 offset:4 sc1\n\t" \
    "s_waitcnt vmcnt(0)\n\t" \
    "v_readfirstlane_b32 s75, v44\n\t" \
    "s_nop 3\n\t" \
    "s_cmp_eq_u32 s75, s80\n\t" \
    "s_cbranch_scc1 LKVOK_%=\n\t" \
    "s_cmp_lt_u32 s74, 0x60000\n\t" \
    "s_cbranch_scc1 LKVP_%=\n\t" \
    "v_mov_b32 v44, s80\n\t" \
    "buffer_store_dword v44, off, %[rs], s76 offset:4 sc1\n\t" \
    "s_waitcnt vmcnt(0)\n\t" \
    "s_branch LKVOK_%=\n\t" \
    "LINS_%=:\n\t" \
    "s_add_u32 s74, s74, 1\n\t" \
    "s_and_b32 s75, s74, 63\n\t" \
    "s_cbranch_scc1 LINR_%=\n\t" \
    "buffer_load_dword v115, off, %[rs], s76 offset:4 sc1\n\t" \
    "s_waitcnt vmcnt(0)\n\t" \
    "v_readfirstlane_b32 s75, v115\n\t" \
    "s_nop 3\n\t" \
    "s_cmp_eq_u32 s75, s80\n\t" \
    "s_cbranch_scc1 LINK_%=\n\t" \
    "s_cmp_lt_u32 s74, 0x60000\n\t" \
    "s_cbranch_scc1 LINR_%=\n\t" \
    "v_mov_b32 v115, s80\n\t" \
    "buffer_store_dword v115, off, %[rs], s76 offset:4 sc1\n\t" \
    "s_waitcnt vmcnt(0)\n\t" \
    "s_branch LINK_%=\n\t" \
    "LINR_%=:\n\t" \
    "buffer_load_dwordx2 v[112:113], v114, %[rs], s76 offen sc1\n\t" \
    "s_waitcnt vmcnt(0)\n\t" \
    "s_branch LIN_%=\n\t" \
    "LG1_%=:\n\t" \
    "s_cmp_lg_u32 s81, 0\n\t" \
    "s_cbranch_scc1 LGA_%=\n\t" \
    "s_waitcnt lgkmcnt(0)\n\t" \
    "buffer_load_dwordx4 v[20:23], %[l16], %[rs], s66 offen sc1\n\t" \
    "buffer_load_dwordx4 v[24:27], %[l16], %[rs], s66 offen offset:1024 sc1\n\t" \
    "s_or_b32 s62, s62, 0x1000000\n\t" \
    "ds_read_b128 v[76:79], %[wob]\n\t" \
    "ds_read_b128 v[80:83], %[wob] offset:1024\n\t" \
    "s_nop 7\n\t" \
    "v_mov_b64 v[52:53], 0\n\t" \
    "v_mov_b64 v[54:55], 0\n\t" \
    "v_mov_b64 v[56:57], 0\n\t" \
    "v_mov_b64 v[58:59], 0\n\t" \
    "v_mov_b64 v[60:61], 0\n\t" \
    "v_mov_b64 v[62:63], 0\n\t" \
    "v_mov_b64 v[64:65], 0\n\t" \
    "v_mov_b64 v[66:67], 0\n\t" \
    "v_mov_b64 v[68:69], 0\n\t" \
    "v_mov_b64 v[70:71], 0\n\t" \
    "v_mov_b64 v[72:73], 0\n\t" \
    "v_mov_b64 v[74:75], 0\n\t" \
    "LGC_%=:\n\t" \
    "s_waitcnt vmcnt(2)\n\t" \
    "v_max3_u32 v116, v108, v109, v110\n\t" \
    "v_max3_u32 v116, v111, v112, v116\n\t" \
    "v_max3_u32 v116, v113, v114, v116\n\t" \
    "v_max_u32 v116, v115, v116\n\t" \
    "v_cmp_le_u32 vcc, 0xff810000, v116\n\t" \
    "s_cbranch_vccz LGT_%=\n\t" \
    "LGS_%=:\n\t" \
    "s_add_u32 s74, s74, 1\n\t" \
    "s_and_b32 s75, s74, 63\n\t" \
    "s_cbranch_scc1 LGR_%=\n\t" \
    "s_mov_b64 s[82:83], 0\n\t" \
    "v_cmp_eq_u32 vcc, 0xff81ff81, v108\n\t" \
    "s_or_b64 s[82:83], s[82:83], vcc\n\t" \
    "v_cmp_eq_u32 vcc, 0xff81ff81, v109\n\t" \
    "s_or_b64 s[82:83], s[82:83], vcc\n\t" \
    "v_cmp_eq_u32 vcc, 0xff81ff81, v110\n\t" \
    "s_or_b64 s[82:83], s[82:83], vcc\n\t" \
    "v_cmp_eq_u32 vcc, 0xff81ff81, v111\n\t" \
    "s_or_b64 s[82:83], s[82:83], vcc\n\t" \
    "v_cmp_eq_u32 vcc, 0xff81ff81, v112\n\t" \
    "s_or_b64 s[82:83], s[82:83], vcc\n\t" \
    "v_cmp_eq_u32 vcc, 0xff81ff81, v113\n\t" \
    "s_or_b64 s[82:83], s[82:83], vcc\n\t" \
    "v_cmp_eq_u32 vcc, 0xff81ff81, v114\n\t" \
    "s_or_b64 s[82:83], s[82:83], vcc\n\t" \
    "v_cmp_eq_u32 vcc, 0xff81ff81, v115\n\t" \
    "s_or_b64 s[82:83], s[82:83], vcc\n\t" \
    "s_cmp_eq_u64 s[82:83], 0\n\t" \
    "s_cbranch_scc1 LGT_%=\n\t" \
    "buffer_load_dword v44, off, %[rs], s76 offset:4 sc1\n\t" \
    "s_waitcnt vmcnt(0)\n\t" \
    "v_readfirstlane_b32 s75, v44\n\t" \
    "s_nop 3\n\t" \
    "s_cmp_eq_u32 s75, s80\n\t" \
    "s_cbranch_scc1 LGT_%=\n\t" \
    "s_cmp_lt_u32 s74, 0x60000\n\t" \
    "s_cbranch_scc1 LGR_%=\n\t" \
    "v_mov_b32 v44, s80\n\t" \
    "buffer_store_dword v44, off, %[rs], s76 offset:4 sc1\n\t" \
    "s_waitcnt vmcnt(0)\n\t" \
    "s_branch LGT_%=\n\t" \
    "LGR_%=:\n\t" \
    "buffer_load_dwordx4 v[108:111], %[l16], %[rs], s66 offen sc1\n\t" \
    "buffer_load_dwordx4 v[112:115], %[l16], %[rs], s66 offen offset:1024 sc1\n\t" \
    "s_waitcnt vmcnt(2)\n\t" \
    "v_max3_u32 v116, v20, v21, v22\n\t" \
    "v_max3_u32 v116, v23, v24, v116\n\t" \
    "v_max3_u32 v116, v25, v26, v116\n\t" \
    "v_max_u32 v116, v27, v116\n\t" \
    "v_cmp_le_u32 vcc, 0xff810000, v116\n\t" \
    "s_cbranch_vccz LGX_%=\n\t" \
    "buffer_load_dwordx4 v[20:23], %[l16], %[rs], s66 offen sc1\n\t" \
    "buffer_load_dwordx4 v[24:27], %[l16], %[rs], s66 offen offset:1024 sc1\n\t" \
    "s_branch LGC_%=\n\t" \
    "LGT_%=:\n\t" \
    "v_mov_b64 v[20:21], v[108:109]\n\t" \
    "v_mov_b64 v[22:23], v[110:111]\n\t" \
    "v_mov_b64 v[24:25], v[112:113]\n\t" \
    "v_mov_b64 v[26:27], v[114:115]\n\t" \
    "LGX_%=:\n\t" \
    "s_mov_b32 s81, 1\n\t" \
    "s_cmp_lt_u32 s60, 63\n\t" \
    "s_cbranch_scc1 LTOP_%=\n\t" \
    "s_branch LLAST_%=\n\t" \
    "LCS_%=:\n\t" \
    "s_add_u32 s74, s74, 1\n\t" \
    "s_cmp_gt_u32 s74, 0x400\n\t" \
    "s_waitcnt lgkmcnt(0)\n\t" \
    "s_cbranch_scc1 LE0_%=\n\t" \
    "buffer_load_dwordx4 v[72:75], v70, %[rs], s38 offen sc1\n\t" \
    "s_waitcnt vmcnt(0)\n\t" \
    "s_branch LCP_%=\n\t" \
    "LR2_%=:\n\t" \
    "s_or_b32 s62, s62, 0x2000000\n\t" \
    "buffer_load_dwordx4 v[76:79], %[perm], %[rs], s63 offen sc1\n\t" \
    "buffer_load_dwordx4 v[80:83], %[perm], %[rs], s63 offen offset:1024 sc1\n\t" \
    "buffer_load_dwordx4 v[84:87], %[perm], %[rs], s63 offen offset:2048 sc1\n\t" \
    "buffer_load_dwordx4 v[88:91], %[perm], %[rs], s63 offen offset:3072 sc1\n\t" \
    "s_waitcnt vmcnt(4)\n\t" \
    "v_mfma_f32_16x16x32_bf16 v[96:99], v[28:31], v[92:95], 0\n\t" \
    "v_max3_u32 v46, v28, v29, v30\n\t" \
    "v_max_u32 v46, v46, v31\n\t" \
    "v_mfma_f32_16x16x32_bf16 v[96:99], v[32:35], v[92:95], v[96:99]\n\t" \
    "v_max3_u32 v46, v32, v33, v46\n\t" \
    "v_max3_u32 v46, v34, v35, v46\n\t" \
    "v_mfma_f32_16x16x32_bf16 v[96:99], v[36:39], v[92:95], v[96:99]\n\t" \
    "v_max3_u32 v46, v36, v37, v46\n\t" \
    "v_max3_u32 v46, v38, v39, v46\n\t" \
    "v_mfma_f32_16x16x32_bf16 v[96:99], v[40:43], v[92:95], v[96:99]\n\t" \
    "v_max3_u32 v46, v40, v41, v46\n\t" \
    "v_max3_u32 v46, v42, v43, v46\n\t" \
    "v_cmp_le_u32 vcc, 0xff810000, v46\n\t" \
    "s_cbranch_vccz LRC_%=\n\t" \
    "LRS_%=:\n\t" \
    "s_add_u32 s74, s74, 1\n\t" \
    "s_and_b32 s75, s74, 63\n\t" \
    "s_cbranch_scc1 LRR_%=\n\t" \
    "s_mov_b64 s[82:83], 0\n\t" \
    "v_cmp_eq_u32 vcc, 0xff81ff81, v28\n\t" \
    "s_or_b64 s[82:83], s[82:83], vcc\n\t" \
    "v_cmp_eq_u32 vcc, 0xff81ff81, v29\n\t" \
    "s_or_b64 s[82:83], s[82:83], vcc\n\t" \
    "v_cmp_eq_u32 vcc, 0xff81ff81, v30\n\t" \
    "s_or_b64 s[82:83], s[82:83], vcc\n\t" \
    "v_cmp_eq_u32 vcc, 0xff81ff81, v31\n\t" \
    "s_or_b64 s[82:83], s[82:83], vcc\n\t" \
    "v_cmp_eq_u32 vcc, 0xff81ff81, v32\n\t" \
    "s_or_b64 s[82:83], s[82:83], vcc\n\t" \
    "v_cmp_eq_u32 vcc, 0xff81ff81, v33\n\t" \
    "s_or_b64 s[82:83], s[82:83], vcc\n\t" \
    "v_cmp_eq_u32 vcc, 0xff81ff81, v34\n\t" \
    "s_or_b64 s[82:83], s[82:83], vcc\n\t" \
    "v_cmp_eq_u32 vcc, 0xff81ff81, v35\n\t" \
    "s_or_b64 s[82:83], s[82:83], vcc\n\t" \
    "v_cmp_eq_u32 vcc, 0xff81ff81, v36\n\t" \
    "s_or_b64 s[82:83], s[82:83], vcc\n\t" \
    "v_cmp_eq_u32 vcc, 0xff81ff81, v37\n\t" \
    "s_or_b64 s[82:83], s[82:83], vcc\n\t" \
    "v_cmp_eq_u32 vcc, 0xff81ff81, v38\n\t" \
    "s_or_b64 s[82:83], s[82:83], vcc\n\t" \
    "v_cmp_eq_u32 vcc, 0xff81ff81, v39\n\t" \
    "s_or_b64 s[82:83], s[82:83], vcc\n\t" \
    "v_cmp_eq_u32 vcc, 0xff81ff81, v40\n\t" \
    "s_or_b64 s[82:83], s[82:83], vcc\n\t" \
    "v_cmp_eq_u32 vcc, 0xff81ff81, v41\n\t" \
    "s_or_b64 s[82:83], s[82:83], vcc\n\t" \
    "v_cmp_eq_u32 vcc, 0xff81ff81, v42\n\t" \
    "s_or_b64 s[82:83], s[82:83], vcc\n\t" \
    "v_cmp_eq_u32 vcc, 0xff81ff81, v43\n\t" \
    "s_or_b64 s[82:83], s[82:83], vcc\n\t" \
    "s_cmp_eq_u64 s[82:83], 0\n\t" \
    "s_cbranch_scc1 LRC_%=\n\t" \
    "buffer_load_dword v44, off, %[rs], s76 offset:4 sc1\n\t" \
    "s_waitcnt vmcnt(0)\n\t" \
    "v_readfirstlane_b32 s75, v44\n\t" \
    "s_nop 3\n\t" \
    "s_cmp_eq_u32 s75, s80\n\t" \
    "s_cbranch_scc1 LRC_%=\n\t" \
    "s_cmp_lt_u32 s74, 0x60000\n\t" \
    "s_cbranch_scc1 LRR_%=\n\t" \
    "v_mov_b32 v44, s80\n\t" \
    "buffer_store_dword v44, off, %[rs], s76 offset:4 sc1\n\t" \
    "s_waitcnt vmcnt(0)\n\t" \
    "s_branch LRC_%=\n\t" \
    "LRR_%=:\n\t" \
    "buffer_load_dwordx4 v[28:31], %[perm], %[rs], s63 offen sc1\n\t" \
    "buffer_load_dwordx4 v[32:35], %[perm], %[rs], s63 offen offset:1024 sc1\n\t" \
    "buffer_load_dwordx4 v[36:39], %[perm], %[rs], s63 offen offset:2048 sc1\n\t" \
    "buffer_load_dwordx4 v[40:43], %[perm], %[rs], s63 offen offset:3072 sc1\n\t" \
    "s_branch LRP_%=\n\t" \
    "LEND_%=:\n\t" \
    ""
#define CHAIN_CLOBBERS \
    "v14", "v15", "v16", "v17", "v18", "v19", "v20", "v21", "v22", "v23", "v24", "v25", "v26", "v27", "v28", "v29", "v30", "v31", "v32", "v33", "v34", "v35", "v36", "v37", \
    "v38", "v39", "v40", "v41", "v42", "v43", "v44", "v45", "v46", "v47", "v48", "v49", "v50", "v51", "v52", "v53", "v54", "v55", "v56", "v57", "v58", "v59", "v60", "v61", \
    "v62", "v63", "v64", "v65", "v66", "v67", "v68", "v69", "v70", "v71", "v72", "v73", "v74", "v75", "v76", "v77", "v78", "v79", "v80", "v81", "v82", "v83", "v84", "v85", \
    "v86", "v87", "v88", "v89", "v90", "v91", "v92", "v93", "v94", "v95", "v96", "v97", "v98", "v99", "v100", "v101", "v102", "v103", "v104", "v105", "v106", "v107", "v108", "v109", \
    "v110", "v111", "v112", "v113", "v114", "v115", "v116", "v117", "v118", "v119", "v120", "v121", "v122", "v123", "v124", "v125", "v126", "v127", "v128", "v129", "v130", "v131", "v132", "v133", \
    "v134", "v135", "v136", "v137", "v138", "v139", "v140", "v141", "v142", "v143", "v144", "v145", "v146", "v147", "v148", "v149", "v150", "v151", "v152", "v153", "v154", "v155", "v156", "v157", \
    "v158", "v159", "v160", "v161", "v162", "v163", "v164", "v165", "v166", "v167", "v168", "v169", "v170", "v171", "v172", "v173", "v174", "v175", "v176", "v177", "v178", "v179", "v180", "v181", \
    "v182", "v183", "v184", "v185", "v186", "v187", "v188", "v189", "v190", "v191", "v192", "v193", "v194", "v195", "v196", "v197", "v198", "v199", "v200", "v201", "v202", "v203", "v204", "v205", \
    "v206", "v207", "v208", "v209", "v210", "v211", "v212", "v213", "v214", "v215", "v216", "v217", "v218", "v219", "v220", "v221", "v222", "v223", "v224", "v225", "v226", "v227", "v228", "v229", \
    "v230", "v231", "v232", "v233", "v234", "v235", "v236", "v237", "v238", "v239", "v240", "v241", "v242", "v243", "v244", "v245", "v246", "v247", "v248", "v249", "v250", "v251", "v252", "v253", \
    "v254", "v255", "a0", "a1", "a2", "a3", "a4", "a5", "a6", "a7", "a8", "a9", "a10", "a11", "a12", "a13", "a14", "a15", "a16", "a17", "a18", "a19", "a20", "a21", \
    "a22", "a23", "a24", "a25", "a26", "a27", "a28", "a29", "a30", "a31", "a32", "a33", "a34", "a35", "a36", "a37", "a38", "a39", "a40", "a41", "a42", "a43", "a44", "a45", \
    "a46", "a47", "a48", "a49", "a50", "a51", "a52", "a53", "a54", "a55", "a56", "a57", "a58", "a59", "a60", "a61", "a62", "a63", "a64", "a65", "a66", "a67", "a68", "a69", \
    "a70", "a71", "a72", "a73", "a74", "a75", "a76", "a77", "a78", "a79", "a80", "a81", "a82", "a83", "a84", "a85", "a86", "a87", "a88", "a89", "a90", "a91", "a92", "a93", \
    "a94", "a95", "a96", "a97", "a98", "a99", "a100", "a101", "a102", "a103", "a104", "a105", "a106", "a107", "a108", "a109", "a110", "a111", "a112", "a113", "a114", "a115", "a116", "a117", \
    "a118", "a119", "a120", "a121", "a122", "a123", "a124", "a125", "a126", "a127", "a128", "a129", "a130", "a131", "a132", "a133", "a134", "a135", "a136", "a137", "a138", "a139", "a140", "a141", \
    "a142", "a143", "a144", "a145", "a146", "a147", "a148", "a149", "a150", "a151", "a152", "a153", "a154", "a155", "a156", "a157", "a158", "a159", "a160", "a161", "a162", "a163", "a164", "a165", \
    "a166", "a167", "a168", "a169", "a170", "a171", "a172", "a173", "a174", "a175", "a176", "a177", "a178", "a179", "a180", "a181", "a182", "a183", "a184", "a185", "a186", "a187", "a188", "a189", \
    "a190", "a191", "a192", "a193", "a194", "a195", "a196", "a197", "a198", "a199", "a200", "a201", "a202", "a203", "a204", "a205", "a206", "a207", "a208", "a209", "a210", "a211", "a212", "a213", \
    "a214", "a215", "a216", "a217", "a218", "a219", "a220", "a221", "a222", "a223", "a224", "a225", "a226", "a227", "a228", "a229", "a230", "a231", "a232", "a233", "a234", "a235", "a236", "a237", \
    "a238", "a239", "a240", "a241", "a242", "a243", "a244", "a245", "a246", "a247", "a248", "a249", "a250", "a251", "a252", "a253", "a254", "a255", "s38", "s39", "s40", "s41", "s42", "s43", \
    "s44", "s45", "s46", "s47", "s48", "s49", "s50", "s51", "s52", "s53", "s54", "s55", "s56", "s57", "s58", "s59", "s60", "s61", "s62", "s63", "s64", "s65", "s66", "s67", \
    "s68", "s69", "s70", "s71", "s72", "s73", "s74", "s75", "s76", "s77", "s78", "s79", "s80", "s81", "s82", "s83", "s84", "s85", "s86", "s87", "s88", "s89", "s90", "s91", \
    "s92", "s93", "s94", "s95", "s96", "s97", "s98", "s99", "vcc", "scc", "memory"
constexpr int W_MEM = 0, W_KV = 8192, W_QK = 24576, W_O = 28672, W_SEL = 30720, W_DUMMY = 31744, W_WL = 32768;
constexpr u32 OOB = 0x40000000u;

__device__ __forceinline__ void chain_worker(const Params& p, LAS char* lds, const int h, const int j, const int lane, const int wave) {
    char* ws = p.ws;
    const int wx = wave ^ (2 * j);
    const rsrc_t rs = __builtin_amdgcn_make_buffer_rsrc(ws, 0, WS_USED, 0x00020000);
    const u32 wofs = (u32)(h * 256 + wave * 64), oofs = (u32)(h * 131072 + j * 512 + wave * 128);
    const rsrc_t rq = __builtin_amdgcn_make_buffer_rsrc((char*)p.Wq + wofs, 0, 4 * MB - wofs, 0x00020000);
    const rsrc_t rk = __builtin_amdgcn_make_buffer_rsrc((char*)p.Wk + wofs, 0, 4 * MB - wofs, 0x00020000);
    const rsrc_t rv = __builtin_amdgcn_make_buffer_rsrc((char*)p.Wv + wofs, 0, 4 * MB - wofs, 0x00020000);
    const rsrc_t ro = __builtin_amdgcn_make_buffer_rsrc((char*)p.Wo + oofs, 0, 4 * MB - oofs, 0x00020000);
    const rsrc_t rm = __builtin_amdgcn_make_buffer_rsrc((void*)p.mem0, 0, 8192, 0x00020000);
    const u32 pol = 0;
    const int g = lane >> 4, c = lane & 15;
    const int row = 2 * (c >> 2) + (c & 1), hi = (c >> 1) & 1;
    const u32 scr = W_WL + wave * 32768;
    const u32 l16 = lane * 16, l8 = (u32)((lane & ~3) + ((lane & 3) ^ g)) * 8, mw = wx * 2048 + l16;
    {
        const u32x4 t0 = {(u32)((lane >> 2) * 8192 + (lane & 3) * 16),
                          scr + (u32)(((lane >> 1) & 1) * 1024 + ((lane >> 4) & 1) * 512 + (lane >> 5) * 128 + ((((lane >> 2) & 3) ^ (lane & 2)) * 32) + (lane & 1) * 16),
                          (u32)((lane >> 3) * 1024 + (lane & 7) * 16),
                          scr + (u32)((lane & 1) * 4160 + ((lane >> 4) & 1) * 512 + (lane >> 5) * 256 + ((lane >> 3) & 1) * 32 + ((lane >> 1) & 3) * 8)};
        const u32x4 t1 = {scr + (u32)lane * 8, (u32)(row * 1024 + wx * 256 + g * 32 + hi * 4),
                          lane < 8 ? C_INIT + (u32)lane * 128u : C_PROG + (u32)(((lane & 1) * 32 + h) * 256 + 128), 0u};
        *(LAS u32x4*)(lds + W_KV + mw) = t0; *(LAS u32x4*)(lds + W_KV + 1024 + mw) = t1;
        u32x4 sel;
#pragma unroll
        for (int i = 0; i < 4; ++i) sel[i] = ((c & 7) == 2 * i) ? 0x00003F80u : (((c & 7) == 2 * i + 1) ? 0x3F800000u : 0u);
        if (wave == 0) *(LAS u32x4*)(lds + W_SEL + l16) = sel;
    }
    const u32 sidx = hi ? 0xEEEEu : 0x4444u;
    const u32 perm = (u32)(g * 256 + hi * 128 + row * 16);
    const u32 pst = comm_off(L_PART + (u32)(((2 * (16 * j + 4 * wave + (c >> 2)) + (g >> 1)) * 32 + h) * 128 + (g & 1) * 64 + (c & 3) * 16), pol);
    const int R = 2 * h + j;
    const u32 mst = comm_off(L_MEM + (u32)(R * 128 + (2 * g + (c & 1)) * 16 + ((c >> 1) & 3) * 4), pol);
    const u32 qkw = (u32)((wave * 32 + c) * 32 + (g ^ (c >> 2)) * 8);
    const u32 ow = hi ? (u32)(W_DUMMY - W_O) + (l16 ^ (u32)((wave & 1) * 32)) : (u32)((wave >> 1) * 1024 + g * 256 + (c + 2 * (wave & 1)) * 16);
    const u32 mr1 = (u32)((1 ^ wx) * 2048) + l16, mr2 = (u32)((2 ^ wx) * 2048) + l16, mr3 = (u32)((3 ^ wx) * 2048) + l16;
    const u32 wob = scr + l16;
    const u32 srld = comm_off(L_PART + (u32)R * 4096u, pol);
    const u32 sca = (u32)(R * 64 + wave * 4);
    const u32 smld = comm_off(L_MEM + (u32)wx * 2048u, pol);
    const u32 skm = ((wave == 0) == (j == 1)) ? OFF_KM + (u32)(h * 262144 + wave * 1024) : OOB;
    const u32 skv = (wave < 2 ? OFF_KF : OFF_VF) + (u32)(h * 8192 + ((wave & 1) ^ (wave >> 1 & j)) * 4096);
    const u32 sflg = OFF_CTRL + C_PROG + (u32)((((wave & 1) * 32 + h) * 2) * 128);
    const u32 skmf = wave == 2 ? OFF_CTRL + C_KM + (u32)(h * 128 + j * 64) : OOB;
    asm volatile(CHAIN_ASM
        :
        : [l16] "v"(l16), [l8] "v"(l8), [perm] "v"(perm), [sidx] "v"(sidx), [pst] "v"(pst), [mst] "v"(mst), [qkw] "v"(qkw), [ow] "v"(ow), [mw] "v"(mw), [wob] "v"(wob),
          [mr1] "v"(mr1), [mr2] "v"(mr2), [mr3] "v"(mr3), [rs] "s"(rs), [rq] "s"(rq), [rk] "s"(rk), [rv] "s"(rv), [ro] "s"(ro), [rm] "s"(rm), [wv] "s"(wave),
          [srld] "s"(srld), [sca] "s"(sca), [smld] "s"(smld), [skm] "s"(skm), [skv] "s"(skv), [sflg] "s"(sflg), [skmf] "s"(skmf)
        : CHAIN_CLOBBERS);
}

template <int SWAP, int PACKT>
__device__ __forceinline__ void proj_loop(const Params& p, const Ctl& k, LAS char* lds, const u32x4 (&W)[4][16], u32x4 (&xn)[16], rsrc_t rx, u32 xo, u32 xstep, rsrc_t rs, u32 obase, u32 ostep, u32 foff, u32 fb, int nit, float osc, int lane, int wave) {
    const int g = lane >> 4, c = lane & 15;
    LAS char* wr[16]; LAS char* rd[4];
#pragma unroll
    for (int i = 0; i < 16; ++i) wr[i] = lds + (wave * 16 + i) * 1024 + ((lane ^ i) * 16);
#pragma unroll
    for (int b = 0; b < 4; ++b) rd[b] = lds + (wave >> 1) * 32768 + c * 1024 + (((4 * b + g) ^ c) * 16);
#pragma unroll
    for (int i = 0; i < 16; ++i) { *(LAS u32x4*)wr[i] = xn[i]; xn[i] = __builtin_amdgcn_raw_buffer_load_b128(rx, xo + i * 1024, xstep, 0); }
    for (int it = 0; it < nit; ++it) {
        if (it == 1) drain(); else if (it >= 2) asm volatile("s_waitcnt vmcnt(20)" ::: "memory");
        bar();
        if ((it == 1 || it >= 3) && wave == 0 && lane < 16) put_flag(k, foff + lane * 8, fb + (u32)(it == 1 ? 2 : 2 * it - 2));
        const int ro = (it & 1) * 65536, wo = 65536 - ro;
        f32x4 acc[4][2];
        u32x4 xf_[3][2];
#pragma unroll
        for (int k = 0; k < 2; ++k)
#pragma unroll
            for (int T = 0; T < 2; ++T) xf_[k][T] = *(LAS u32x4*)(rd[k] + ro + T * 16384);
        const u32 so = (u32)(it + 2) * xstep;
        SB();
#pragma unroll
        for (int ks = 0; ks < 16; ++ks) {
#pragma unroll
            for (int T = 0; T < 2; ++T) {
#pragma unroll
                for (int t = 0; t < 4; ++t) {
                    const f32x4 ci = ks ? acc[t][T] : (f32x4){0.f, 0.f, 0.f, 0.f};
                    acc[t][T] = SWAP ? mfma32(xf_[ks % 3][T], W[t][ks], ci) : mfma32(W[t][ks], xf_[ks % 3][T], ci);
                    if (t == 0 && ks + 2 < 16) xf_[(ks + 2) % 3][T] = *(LAS u32x4*)(rd[(ks + 2) & 3] + ro + T * 16384 + ((ks + 2) >> 2) * 256);
                    if (t == 2 && T == 0) *(LAS u32x4*)(wr[ks] + wo) = xn[ks];
                    if (t == 2 && T == 1) xn[ks] = __builtin_amdgcn_raw_buffer_load_b128(rx, xo + ks * 1024, so, 0);
                    SB();
                }
            }
        }
        const u32 o = obase + (u32)it * ostep + lane * 16;
        if (!SWAP) {
#pragma unroll
            for (int t = 0; t < 4; ++t) { acc[t][0] *= osc; acc[t][1] *= osc; }
        }
#pragma unroll
        for (int j = 0; j < 2; ++j) {
            if (PACKT) { st16(rs, o + j * 2048, pk8(acc[2 * j][0], acc[2 * j][1])); st16(rs, o + j * 2048 + 1024, pk8(acc[2 * j + 1][0], acc[2 * j + 1][1])); }
            else { st16(rs, o + j * 1024, pk8(acc[2 * j][0], acc[2 * j + 1][0])); st16(rs, o + j * 1024 + 4096, pk8(acc[2 * j][1], acc[2 * j + 1][1])); }
        }
    }
    drain();
    bar();
    if (wave == 0 && lane < 16) put_flag(k, foff + lane * 8, fb + (u32)(2 * nit));
}

__device__ __forceinline__ void proj_helper(const Params& p, Ctl& k, u32 call, LAS char* lds, int sl, int tp, int lane, int wave) {
    const int pstep = sl < 96 ? 1 : 2, nit = sl < 64 ? 30 : (sl < 96 ? 32 : 16), p0 = sl < 64 ? 2 : tp;
    const rsrc_t rs = __builtin_amdgcn_make_buffer_rsrc(p.ws, 0, WS_USED, 0x00020000);
    const u16* src; int ld;
    if (sl < 96) { src = (sl < 32 ? p.Wk : (sl < 64 ? p.Wv : p.Wq)) + (sl & 31) * 128 + wave * 32; ld = 4096; }
    else { src = p.wqkv + (sl - 96) * 128 + wave * 32; ld = 1536; }
    u32x4 W[4][16], xn[16];
    const rsrc_t rx = __builtin_amdgcn_make_buffer_rsrc((void*)p.x, 0, 2 * MB, 0x00020000);
    const u32 xo = (u32)((p0 * 64 + wave * 16) * 1024 + lane * 16);
    {
        u32x4 st[32];
        stage32_load<0, 32>(src, ld, lane, st, 0);
#pragma unroll
        for (int i = 0; i < 16; ++i) xn[i] = __builtin_amdgcn_raw_buffer_load_b128(rx, xo + i * 1024, 0, 0);
        SB();
        k.tag = __builtin_amdgcn_readfirstlane(call) + 1u;
        stage32_write<0, 32>(lds + wave * 32768, lane, st);
        bar();
#pragma unroll
        for (int ks = 0; ks < 16; ++ks)
#pragma unroll
            for (int t = 0; t < 4; ++t) { W[t][ks] = stage32_frag(lds + ((wave & 1) * 2 + (t >> 1)) * 32768, lane, ks, t & 1); if (ks < 13) asm volatile("" : "+a"(W[t][ks])); }
    }
    bar();
    const u32 foff = C_PROG + (u32)((sl * 2 + tp) * 128);
    const int ch = wave & 1, sg = p0 * 2 + (wave >> 1);
    const u32 fb = sl < 64 ? 4u : 0u;
    const u32 xstep = (u32)pstep * 65536u;
    const float osc = (sl >= 64 && sl < 100) ? (sl < 96 ? 0.08838834764831845f : 0.25f) * 1.4426950408889634f : 1.f;
    if (sl < 32 || (sl >= 64 && sl < 96)) proj_loop<0, 0>(p, k, lds, W, xn, rx, xo, xstep, rs, (sl < 32 ? OFF_KF : OFF_QF) + (sg * 32 + (sl & 31)) * 8192 + ch * 2048, 64 * 8192, foff, fb, nit, osc, lane, wave);
    else if (sl >= 96 && sl < 104) proj_loop<0, 1>(p, k, lds, W, xn, rx, xo, xstep, rs, (sl < 100 ? OFF_QA : OFF_KA) + (sg * 32 + (sl & 3) * 8 + ch * 4) * 1024, 128 * 1024, foff, fb, nit, osc, lane, wave);
    else proj_loop<1, 1>(p, k, lds, W, xn, rx, xo, xstep, rs, sl < 64 ? OFF_VF + (sg * 32 + (sl & 31)) * 8192 + ch * 4096 : OFF_VA + (sg * 32 + (sl & 3) * 8 + ch * 4) * 1024, sl < 64 ? 64 * 8192 : 128 * 1024, foff, fb, nit, osc, lane, wave);
    bar();
}

template <int ISV>
__device__ __forceinline__ void first_helper(const Params& p, LAS char* lds, rsrc_t rs, int h, int lane, int wave) {
    const int g = lane >> 4, c = lane & 15;
    const u32 ct = code_touch<true>(wave, lane);
    Ctl k; k.base = p.ws + OFF_CTRL;
    const u32 call = __hip_atomic_load((gu32*)(k.base + C_CALL), RLX_AGENT);
    const u16* src = (ISV ? p.Wv : p.Wk) + h * 128 + wave * 32 + (size_t)(lane >> 2) * 4096 + (lane & 3) * 8;
    const u16* xs = p.x + (size_t)(wave * 16) * 512 + lane * 8;
    u32x4 st[4][8], xr[16];
    auto wload = [&](int j) {
#pragma unroll
        for (int i = 0; i < 8; ++i) st[j][i] = *(const u32x4*)(src + (size_t)(((((j & 1) * 4 + (j >> 1) + 2 * (i >> 2)) ^ (2 * wave)) * 64 + (i & 3) * 16) * 4096));
    };
#pragma unroll
    for (int i = 0; i < 16; ++i) xr[i] = *(const u32x4*)(xs + i * 512);
    wload(0);
    SB();
#pragma unroll
    for (int i = 0; i < 16; ++i) { *(LAS u32x4*)(lds + (wave * 16 + i) * 1024 + ((lane ^ (i & 15)) * 16)) = xr[i]; if (i == 7) { SB(); wload(1); SB(); } }
    k.tag = __builtin_amdgcn_readfirstlane(call) + 1u;
    asm volatile("" :: "v"(ct));
    bar();
    LAS char* scr = lds + 131072 + wave * 8192;
    u32x4 W[16][2];
    f32x4 acc[2][4];
    u32x4 xf_[2][4][4];
    auto xread = [&](int j, int pass) {
#pragma unroll
        for (int q = 0; q < 4; ++q) {
            const int ks = (((j & 1) * 4 + (j >> 1) + 2 * (q >> 1)) ^ (2 * wave)) * 2 + (q & 1);
            LAS char* xb = lds + pass * 65536 + c * 1024 + (((4 * ks + g) ^ c) * 16);
#pragma unroll
            for (int T = 0; T < 4; ++T) xf_[j & 1][q][T] = *(LAS u32x4*)(xb + T * 16384);
        }
    };
    auto mm = [&](int j) {
#pragma unroll
        for (int q = 0; q < 4; ++q)
#pragma unroll
            for (int T = 0; T < 4; ++T)
#pragma unroll
                for (int t = 0; t < 2; ++t) {
                    const f32x4 ci = (j | q) ? acc[t][T] : (f32x4){0.f, 0.f, 0.f, 0.f};
                    acc[t][T] = ISV ? mfma32(xf_[j & 1][q][T], W[j * 4 + q][t], ci) : mfma32(W[j * 4 + q][t], xf_[j & 1][q][T], ci);
                }
    };
    auto publish = [&](int pass) {
#pragma unroll
        for (int sg = 0; sg < 2; ++sg) {
            const u32 o = (u32)(((pass * 2 + sg) * 32 + h) * 8192 + lane * 16);
            if (ISV) { st16(rs, OFF_VF + o + wave * 2048, pk8(acc[0][2 * sg], acc[0][2 * sg + 1])); st16(rs, OFF_VF + o + wave * 2048 + 1024, pk8(acc[1][2 * sg], acc[1][2 * sg + 1])); }
            else { st16(rs, OFF_KF + o + wave * 1024, pk8(acc[0][2 * sg], acc[1][2 * sg])); st16(rs, OFF_KF + o + wave * 1024 + 4096, pk8(acc[0][2 * sg + 1], acc[1][2 * sg + 1])); }
        }
    };
#pragma unroll
    for (int j = 0; j < 4; ++j) {
        if (j + 2 < 4) { wload(j + 2); SB(); }
        xread(j, 0);
        SB();
        stage32_write<0, 8>(scr, lane, st[j]);
#pragma unroll
        for (int q = 0; q < 4; ++q) { W[j * 4 + q][0] = stage32_frag(scr, lane, q, 0); W[j * 4 + q][1] = stage32_frag(scr, lane, q, 1); }
        SB();
        mm(j);
#pragma unroll
        for (int q = 0; q < 4; ++q) asm volatile("" : "+a"(W[j * 4 + q][0]), "+a"(W[j * 4 + q][1]));
        if (j == 2) {
#pragma unroll
            for (int i = 0; i < 16; ++i) xr[i] = *(const u32x4*)(xs + (64 + i) * 512);
        }
        SB();
    }
    publish(0);
    drain();
    bar();
    if (wave == 0 && lane < 16) put_flag(k, C_PROG + (u32)((ISV * 32 + h) * 256 + 128) + lane * 8, 2u);
#pragma unroll
    for (int i = 0; i < 16; ++i) *(LAS u32x4*)(lds + 65536 + (wave * 16 + i) * 1024 + ((lane ^ i) * 16)) = xr[i];
    bar();
    xread(0, 1);
#pragma unroll
    for (int j = 0; j < 4; ++j) { if (j < 3) xread(j + 1, 1); SB(); mm(j); SB(); }
    publish(1);
    drain();
    bar();
    if (wave == 0 && lane < 16) put_flag(k, C_PROG + (u32)((ISV * 32 + h) * 256 + 128) + lane * 8, 4u);
}

struct AIn { u32x4 q, k[9], v[9]; };
__device__ __forceinline__ void attnA_load(AIn& d, rsrc_t rs, int t, int lane) {
    const int qs = t >> 5;
    const u32 hb = (u32)(t & 31) * 1024u;
    d.q = ld16s(rs, lane * 16, OFF_QA + (u32)qs * 32768u + hb);
#pragma unroll
    for (int i = 0; i < 9; ++i) { const u32 o = (u32)max(qs - 8 + i, 0) * 32768u + hb; d.k[i] = ld16s(rs, lane * 16, OFF_KA + o); d.v[i] = ld16s(rs, lane * 16, OFF_VA + o); }
}
#define MF16 "v_mfma_f32_16x16x16_bf16 "
__device__ __forceinline__ void score8(f32x4* s, const u32x2* kf, u32x2 q, f32x4 c0) {
    asm volatile("s_nop 1\n\t" MF16 "%0, %8, %16, %17\n\t" MF16 "%1, %9, %16, 0\n\t" MF16 "%2, %10, %16, 0\n\t" MF16 "%3, %11, %16, 0\n\t"
                 MF16 "%4, %12, %16, 0\n\t" MF16 "%5, %13, %16, 0\n\t" MF16 "%6, %14, %16, 0\n\t" MF16 "%7, %15, %16, 0\n\ts_nop 7"
                 : "=&v"(s[0]), "=&v"(s[1]), "=&v"(s[2]), "=&v"(s[3]), "=&v"(s[4]), "=&v"(s[5]), "=&v"(s[6]), "=&v"(s[7])
                 : "v"(kf[0]), "v"(kf[1]), "v"(kf[2]), "v"(kf[3]), "v"(kf[4]), "v"(kf[5]), "v"(kf[6]), "v"(kf[7]), "v"(q), "v"(c0));
}
__device__ __forceinline__ f32x4 score1(u32x2 kf, u32x2 q, f32x4 c0) {
    f32x4 s; asm volatile("s_nop 1\n\t" MF16 "%0, %1, %2, %3\n\ts_nop 7" : "=&v"(s) : "v"(kf), "v"(q), "v"(c0)); return s;
}
__device__ __forceinline__ float max3(float a, float b, float c) { float r; asm("v_max3_f32 %0, %1, %2, %3" : "=v"(r) : "v"(a), "v"(b), "v"(c)); return r; }
template <bool EDGE>
__device__ __forceinline__ void attnA_compute(const AIn& d, rsrc_t rs, int t, int lane) {
    const int g = lane >> 4, c = lane & 15, qs = t >> 5, hd = t & 31;
    const f32x4 z4 = {0.f, 0.f, 0.f, 0.f};
    const float NEG = -__builtin_inff();
    f32x4 lo, up;
#pragma unroll
    for (int r = 0; r < 4; ++r) { lo[r] = (c >= 4 * g + r) ? 0.f : NEG; up[r] = (c >= 4 * g + r) ? NEG : 0.f; }
    const u32x4 ones = {0x3F803F80u, 0x3F803F80u, 0x3F803F80u, 0x3F803F80u};
#pragma unroll
    for (int T = 0; T < 2; ++T) {
        const u32x2 qf = {d.q[2 * T], d.q[2 * T + 1]};
        u32x2 kf[17];
#pragma unroll
        for (int n = 0; n < 17; ++n) { const int m = n + T; kf[n] = (u32x2){d.k[m >> 1][2 * (m & 1)], d.k[m >> 1][2 * (m & 1) + 1]}; }
        f32x4 s[18];
        f32x4* sv = s + T;
        score8(sv, kf, qf, up); score8(sv + 8, kf + 8, qf, z4); sv[16] = score1(kf[16], qf, lo);
        if (EDGE) {
#pragma unroll
            for (int n = 0; n < 16; ++n) if (qs - 8 + ((n + T) >> 1) < 0) sv[n] = (f32x4){NEG, NEG, NEG, NEG};
        }
        float mx = NEG, my = NEG;
#pragma unroll
        for (int n = 0; n < 17; ++n) { mx = max3(mx, sv[n][0], sv[n][1]); my = max3(my, sv[n][2], sv[n][3]); }
        mx = allmax_g(fmaxf(mx, my));
#pragma unroll
        for (int n = 0; n < 17; ++n) sv[n] = sv[n] - mx;
        SB();
        s[T ? 0 : 17] = z4;
        f32x4 o = z4, l = z4;
#pragma unroll
        for (int i = 0; i < 9; ++i) {
#pragma unroll
            for (int U = 0; U < 2; ++U) {
                if (2 * i + U == (T ? 0 : 17)) continue;
#pragma unroll
                for (int r = 0; r < 4; ++r) s[2 * i + U][r] = __builtin_amdgcn_exp2f(s[2 * i + U][r]);
            }
            const u32x4 pp = pk8(s[2 * i], s[2 * i + 1]);
            o = mfma32(d.v[i], pp, o);
            l = mfma32(ones, pp, l);
        }
        const float rl = __builtin_amdgcn_rcpf(l[0]);
        st8(rs, OFF_OA + (u32)(((qs * 2 + T) * 32 + hd) * 512 + lane * 8), pk4(o * rl));
    }
}
__device__ __forceinline__ void attnA_run(const Ctl& k, rsrc_t rs, int t0, int tstep, int tend, int lane) {
    AIn a, b;
    auto signal = [&](int t) { if (lane == 0) put_flag(k, C_DONEA + (u32)t * 8u, 1u); };
    auto step = [&](AIn& cur, AIn& nxt, int t) {
        asm volatile("s_waitcnt vmcnt(2)" ::: "memory");
        if (t >= t0 + 2 * tstep) signal(t - 2 * tstep);
        attnA_load(nxt, rs, min(t + tstep, 2047), lane);
        SB();
        if (t < 256) attnA_compute<true>(cur, rs, t, lane); else attnA_compute<false>(cur, rs, t, lane);
    };
    attnA_load(a, rs, t0, lane);
    int t = t0;
    for (;;) {
        step(a, b, t); t += tstep; if (t >= tend) break;
        step(b, a, t); t += tstep; if (t >= tend) break;
    }
    drain();
    if (t >= t0 + 2 * tstep) signal(t - 2 * tstep);
    signal(t - tstep);
}

struct BIn { u32x4 q[8], k[8], v[8]; };
__device__ __forceinline__ void attnB_load(BIn& d, rsrc_t rs, int s, int h, int lane) {
    const u32 fo = (u32)((s * 32 + h) * 8192);
#pragma unroll
    for (int i = 0; i < 8; ++i) { const u32 vo = lane * 16 + (i & 3) * 1024, so = fo + (i >> 2) * 4096; d.q[i] = ld16s(rs, vo, OFF_QF + so); d.k[i] = ld16s(rs, vo, OFF_KF + so); d.v[i] = ld16s(rs, vo, OFF_VF + so); }
}
__device__ __forceinline__ void attnB_task(const Ctl& k, rsrc_t rs, LAS char* scr, const BIn& d, BIn& nxt, int s, int h, int lane) {
    const int g = lane >> 4, c = lane & 15;
    wait_flags(k, lane < 3 ? C_PROG + (u32)(lane * 32 + h) * 256u : C_KM + (u32)(((s + 1) * 32 + h) * 128 + (lane & 1) * 64), lane < 3 ? (u32)s + 5u : 1u);
    const f32x4 z4 = {0.f, 0.f, 0.f, 0.f};
    f32x4 s1[2] = {z4, z4}, s2 = z4;
    u32x4 km[4], kv[4];
    {
#pragma unroll
        for (int i = 0; i < 4; ++i) kv[i] = ld16(rs, OFF_KM + (u32)(h * 262144 + s * 4096 + i * 1024 + lane * 16));
        attnB_load(nxt, rs, s + 4, h, lane);
        SB();
#pragma unroll
        for (int ks = 0; ks < 4; ++ks) { s1[0] = mfma32(d.k[ks], d.q[ks], s1[0]); s1[1] = mfma32(d.k[ks], d.q[4 + ks], s1[1]); s2 = mfma32(d.k[4 + ks], d.q[4 + ks], s2); }
#pragma unroll
        for (int i = 0; i < 4; ++i) {
            const u32x2 a = {kv[i][0], 0u}, b = {kv[i][1], 0u};
            *(LAS u32x2*)(scr + (i * 32 + c) * 32 + (g ^ (c >> 2)) * 8) = a; *(LAS u32x2*)(scr + (i * 32 + 16 + c) * 32 + (g ^ (c >> 2)) * 8) = b;
        }
#pragma unroll
        for (int i = 0; i < 4; ++i) km[i] = cat(trd(scr + ((lane & ~3) + ((lane & 3) ^ g)) * 8 + i * 1024), trd(scr + ((lane & ~3) + ((lane & 3) ^ g)) * 8 + i * 1024 + 512));
    }
    const float NEG = -__builtin_inff();
#pragma unroll
    for (int T = 0; T < 2; ++T) {
        f32x4 s0 = z4;
#pragma unroll
        for (int ks = 0; ks < 4; ++ks) s0 = mfma32(km[ks], d.q[T * 4 + ks], s0);
        f32x4 a = s1[T], b = s2;
#pragma unroll
        for (int r = 0; r < 4; ++r) {
            const bool ok = c >= 4 * g + r;
            if (T == 0) { a[r] = ok ? a[r] : NEG; b[r] = NEG; } else { b[r] = ok ? b[r] : NEG; }
        }
        float mx = fmaxf(fmaxf(fmaxf(a[0], a[1]), fmaxf(a[2], a[3])), fmaxf(fmaxf(b[0], b[1]), fmaxf(b[2], b[3])));
        mx = fmaxf(mx, fmaxf(s0[0], s0[1]));
        mx = allmax_g(mx);
        const float e0 = __builtin_amdgcn_exp2f(s0[0] - mx), e1 = __builtin_amdgcn_exp2f(s0[1] - mx);
#pragma unroll
        for (int r = 0; r < 4; ++r) {
            a[r] = __builtin_amdgcn_exp2f(a[r] - mx); b[r] = __builtin_amdgcn_exp2f(b[r] - mx);
        }
        float l = ((a[0] + a[1]) + (a[2] + a[3])) + ((b[0] + b[1]) + (b[2] + b[3])) + (e0 + e1);
        l = allsum_g(l);
        const float rl = __builtin_amdgcn_rcpf(l);
        const u32x4 px = pk8(a, b);
        const u32x2 pz = {pk2(e0, e1), 0u}, zp = {0u, pk2(e0, e1)};
#pragma unroll
        for (int u = 0; u < 4; ++u) {
            f32x4 o0 = mfma32(d.v[2 * u], px, z4), o1 = mfma32(d.v[2 * u + 1], px, z4);
            const u32x2 vv = {kv[u][2], kv[u][3]};
            o0 = mfma16(vv, pz, o0); o1 = mfma16(vv, zp, o1);
#pragma unroll
            for (int r = 0; r < 4; ++r) { o0[r] *= rl; o1[r] *= rl; }
            st16(rs, OFF_OF + (u32)(((s * 2 + T) * 128 + h * 4 + u) * 1024 + lane * 16), pk8(o0, o1));
        }
    }
    drain();
    if (lane < 2) put_flag(k, C_DONEB + (u32)((s * 2 + lane) * 32 + h) * 128u, 1u);
}

struct QIn { u32x4 q[4], k[8], v[8]; };
__device__ __forceinline__ void half_load(QIn& d, rsrc_t rs, int s, int h, int T, int lane) {
    const u32 fo = s < 64 ? (u32)((s * 32 + h) * 8192) : OOB;
#pragma unroll
    for (int i = 0; i < 8; ++i) {
        const u32 vo = lane * 16 + (i & 3) * 1024, so = fo + (i >> 2) * 4096;
        if (i < 4) d.q[i] = ld16s(rs, vo, OFF_QF + fo + T * 4096);
        d.k[i] = ld16s(rs, vo, OFF_KF + so); d.v[i] = ld16s(rs, vo, OFF_VF + so);
    }
}
__device__ __forceinline__ void attnB_half(const Ctl& k, rsrc_t rs, LAS char* scr, QIn& d, int s, int h, int T, int lane) {
    const int g = lane >> 4, c = lane & 15;
    const f32x4 z4 = {0.f, 0.f, 0.f, 0.f};
    const float NEG = -__builtin_inff();
    f32x4 a, b;
#pragma unroll
    for (int r = 0; r < 4; ++r) { const bool ok = c >= 4 * g + r; a[r] = (ok || T) ? 0.f : NEG; b[r] = (ok && T) ? 0.f : NEG; }
#pragma unroll
    for (int ks = 0; ks < 4; ++ks) { a = mfma32(d.k[ks], d.q[ks], a); b = mfma32(d.k[4 + ks], d.q[ks], b); }
    float mx = fmaxf(fmaxf(fmaxf(a[0], a[1]), fmaxf(a[2], a[3])), fmaxf(fmaxf(b[0], b[1]), fmaxf(b[2], b[3])));
    asm volatile("" : "+v"(mx), "+v"(a), "+v"(b));
    wait_flags(k, C_KM + (u32)(((s + 1) * 32 + h) * 128 + (lane & 1) * 64), 1u);
    u32x4 kv[4];
#pragma unroll
    for (int i = 0; i < 4; ++i) kv[i] = ld16s(rs, lane * 16 + i * 1024, OFF_KM + (u32)(h * 262144 + s * 4096));
    SB();
    LAS char* wp = scr + c * 32 + (g ^ (c >> 2)) * 8;
    LAS char* rp = scr + ((lane & ~3) + ((lane & 3) ^ g)) * 8;
    f32x4 s0 = z4;
#pragma unroll
    for (int i = 0; i < 4; ++i) {
        const u32x2 k0 = {kv[i][0], kv[i][1]}, k1 = {kv[i][1], kv[i][2]};
        *(LAS u32x2*)(wp + i * 1024) = k0; *(LAS u32x2*)(wp + i * 1024 + 512) = k1;
        s0 = mfma32(cat(trd(rp + i * 1024), trd(rp + i * 1024 + 512)), d.q[i], s0);
    }
    mx = allmax_g(fmaxf(mx, fmaxf(s0[0], s0[1])));
    const float e0 = __builtin_amdgcn_exp2f(s0[0] - mx), e1 = __builtin_amdgcn_exp2f(s0[1] - mx);
#pragma unroll
    for (int r = 0; r < 4; ++r) { a[r] = __builtin_amdgcn_exp2f(a[r] - mx); b[r] = __builtin_amdgcn_exp2f(b[r] - mx); }
    const u32x4 px = pk8(a, b);
    const u32x2 pz = {pk2(e0, e1), 0u}, zp = {0u, pk2(e0, e1)};
    float l = ((a[0] + a[1]) + (a[2] + a[3])) + ((b[0] + b[1]) + (b[2] + b[3])) + (e0 + e1);
    const float rl = __builtin_amdgcn_rcpf(allsum_g(l));
#pragma unroll
    for (int u = 0; u < 4; ++u) {
        const u32x2 vv = {kv[u][2], kv[u][3]};
        const f32x4 o0 = mfma16(vv, pz, mfma32(d.v[2 * u], px, z4)), o1 = mfma16(vv, zp, mfma32(d.v[2 * u + 1], px, z4));
        st16(rs, OFF_OF + (u32)(((s * 2 + T) * 128 + h * 4 + u) * 1024 + lane * 16), pk8(o0 * rl, o1 * rl));
    }
    drain();
    if (lane == 0) put_flag(k, C_DONEB + (u32)((s * 2 + T) * 32 + h) * 128u, 1u);
    half_load(d, rs, s + 2, h, T, lane);
}

__device__ __forceinline__ void out_helper(const Params& p, const Ctl& k, LAS char* lds, int cg, int set, int lane, int wave) {
    const rsrc_t rs = __builtin_amdgcn_make_buffer_rsrc(p.ws, 0, WS_USED, 0x00020000);
    const int g = lane >> 4, c = lane & 15;
    u32x4 WB[2][32];
    LAS char* wa = lds + 131072 + wave * 8192;
    {
        LAS char* scr = lds + wave * 32768;
#pragma unroll
        for (int hf = 0; hf < 64 / WCH; ++hf) {
            u32x4 st[WCH];
            stage32_load<1, WCH>(p.Wo + (size_t)(wave * 1024 + hf * WCH * 16) * 512 + cg * 32, 512, lane, st, 0);
            SB();
            asm volatile("s_waitcnt lgkmcnt(0)" ::: "memory");
            stage32_write<1, WCH>(scr, lane, st);
#pragma unroll
            for (int ks = 0; ks < WCH / 2; ++ks)
#pragma unroll
                for (int t = 0; t < 2; ++t) { WB[t][hf * (WCH / 2) + ks] = stage32_frag(scr, lane, ks, t); if (hf * (WCH / 2) + ks < 29) asm volatile("" : "+a"(WB[t][hf * (WCH / 2) + ks])); }
            SB();
        }
        u32x4 st[8];
        stage32_load<1, 8>(p.wo + (size_t)(wave * 128) * 512 + cg * 32, 512, lane, st, 0);
        stage32_write<1, 8>(wa, lane, st);
    }
    bar();
    const f32x4 z4 = {0.f, 0.f, 0.f, 0.f};
    int par = 0;
#pragma unroll 1
    for (int i = 0; i < 8; ++i) wait_flags<32>(k, C_DONEA + (u32)((i * 8 + (lane >> 3)) * 256 + (wave * 8 + (lane & 7)) * 8), 1u);
    const rsrc_t ro = __builtin_amdgcn_make_buffer_rsrc(p.out, 0, 2 * MB, 0x00020000);
    u32x4 oa[4], ob[32]; float gn;
    auto tile_of = [](int u) { return u < 126 ? u : 126 + ((u - 126) >> 1); };
    auto flag_of = [&](int u) { return C_DONEB + (u32)(tile_of(u) * 32 + wave * 8 + (lane & 7)) * 128u; };
    auto load_a = [&](int u) {
        const int tile = tile_of(u);
        gn = fx(__builtin_amdgcn_raw_buffer_load_b32(rs, (u32)((wave & 1) * 8 + (lane >> 3)) * 4u, OFF_GATE + (u32)tile * 64u, 16));
#pragma unroll
        for (int ks = 0; ks < 4; ++ks)
            oa[ks] = cat(__builtin_amdgcn_raw_buffer_load_b64(rs, lane * 8 + ks * 1024, OFF_OA + (u32)((tile * 16 + wave * 4) * 1024), 16), __builtin_amdgcn_raw_buffer_load_b64(rs, lane * 8 + ks * 1024 + 512, OFF_OA + (u32)((tile * 16 + wave * 4) * 1024), 16));
    };
    auto load_b = [&](int u) {
        const int tile = tile_of(u);
        const u32 vo = (u < 126 || (c >> 3) == (u & 1)) ? (u32)lane * 16u : OOB;
#pragma unroll
        for (int ks = 0; ks < 32; ++ks) ob[ks] = ld16s(rs, vo + (ks & 3) * 1024, OFF_OF + (u32)((tile * 128 + wave * 32 + (ks & ~3)) * 1024));
        SB();
    };
    load_a(set); wait_flags(k, flag_of(set), 1u); load_b(set);
    for (int u = set; u < 130; u += 6, par ^= 1) {
        const int un = min(u + 6, 129);
        const unsigned long long fn = __hip_atomic_load((gu64*)(k.base + flag_of(un)), RLX_AGENT);
        SB();
        f32x4 aa0 = z4, aa1 = z4, ab0 = z4, ab1 = z4;
#pragma unroll
        for (int ks = 0; ks < 4; ++ks) { aa0 = mfma32(stage32_frag(wa, lane, ks, 0), oa[ks], aa0); aa1 = mfma32(stage32_frag(wa, lane, ks, 1), oa[ks], aa1); }
#pragma unroll
        for (int ks = 0; ks < 32; ++ks) { ab0 = mfma32(WB[0][ks], ob[ks], ab0); ab1 = mfma32(WB[1][ks], ob[ks], ab1); }
        float gt = gn;
        asm volatile("" : "+v"(gt));
        auto finish = [&]() {
            LAS char* red = lds + par * 16384;
            *(LAS f32x4*)(red + (0 * 4 + wave) * 1024 + lane * 16) = aa0;
            *(LAS f32x4*)(red + (1 * 4 + wave) * 1024 + lane * 16) = aa1;
            *(LAS f32x4*)(red + (2 * 4 + wave) * 1024 + lane * 16) = ab0;
            *(LAS f32x4*)(red + (3 * 4 + wave) * 1024 + lane * 16) = ab1;
            bar();
            if (wave < 2 && (u < 126 || wave == (u & 1))) {
                const int tk = wave * 8 + (lane >> 3), t = (lane >> 2) & 1, src = ((lane & 3) * 16 + tk) * 16;
                f32x4 ra[4];
#pragma unroll
                for (int w = 0; w < 4; ++w) ra[w] = *(LAS f32x4*)(red + (t * 4 + w) * 1024 + src);
                SB();
                const f32x4 a = (ra[0] + ra[1]) + (ra[2] + ra[3]);
#pragma unroll
                for (int w = 0; w < 4; ++w) ra[w] = *(LAS f32x4*)(red + ((2 + t) * 4 + w) * 1024 + src);
                SB();
                const f32x4 b = (ra[0] + ra[1]) + (ra[2] + ra[3]);
                const float og = rb(1.f - gt);
                f32x4 o;
#pragma unroll
                for (int r = 0; r < 4; ++r) o[r] = rb(gt * rb(a[r])) + rb(og * rb(b[r]));
                __builtin_amdgcn_raw_buffer_store_b64(pk4(o), ro, (u32)(tk * 1024 + (lane & 7) * 8), (u32)(tile_of(u) * 16384 + cg * 64), 0);
            }
        };
        if (u + 6 >= 130) { finish(); break; }
        if (__all((u32)(fn >> 32) == k.tag && (u32)fn == 1u)) { fence_c(); load_a(un); load_b(un); finish(); }
        else { finish(); load_a(un); wait_flags(k, flag_of(un), 1u); load_b(un); }
    }
}

__global__ void __launch_bounds__(256, 1) main_kernel(Params p) {
    extern __shared__ __attribute__((aligned(16))) char smem[];
    LAS char* lds = (LAS char*)smem;
    const int b = blockIdx.x, lane = threadIdx.x & 63, wave = __builtin_amdgcn_readfirstlane(threadIdx.x >> 6);
    const int bx = b & 7, by = b >> 3;
    if (bx < 4 && by < 16) {
        chain_worker(p, lds, (by >> 1) * 4 + bx, by & 1, lane, wave);
        return;
    }
    const rsrc_t rs = __builtin_amdgcn_make_buffer_rsrc(p.ws, 0, WS_USED, 0x00020000);
    if (bx < 4) {
        if (by < 24) first_helper<0>(p, lds, rs, (by - 16) * 4 + bx, lane, wave);
        else first_helper<1>(p, lds, rs, (by - 24) * 4 + bx, lane, wave);
        return;
    }
    const int idx = by * 4 + (bx - 4);
    Ctl k; k.base = p.ws + OFF_CTRL;
    const u32 ct = code_touch<false>((by & 15) * 4 + wave, lane);
    const u32 call = __hip_atomic_load((gu32*)(k.base + C_CALL), RLX_AGENT);
    if (idx >= 96 && idx < 120) {
        const rsrc_t rt = __builtin_amdgcn_make_buffer_rsrc((void*)p.Wo, 0, 4 * MB, 0x00020000);
        u32x4 st[43];
#pragma unroll
        for (int i = 0; i < 43; ++i) { const u32 n = (u32)(i * 96 + (idx - 96) * 4 + wave); st[i] = __builtin_amdgcn_raw_buffer_load_b128(rt, lane * 16, n < 4096u ? (((n & 2047u) >> 6) * 128u + (n >> 11) * 64u + (n & 63u)) * 1024u : OOB, 0); }
        SB();
#pragma unroll
        for (int i = 0; i < 43; ++i) asm volatile("" :: "v"(st[i]));
    }
    if (idx < 120) proj_helper(p, k, call, lds, idx < 96 ? idx : 96 + ((idx - 96) >> 1), idx < 96 ? 0 : (idx & 1), lane, wave);
    else { k.tag = __builtin_amdgcn_readfirstlane(call) + 1u; setup_helper(p, k, lds, rs, idx - 120, lane, wave); }
    asm volatile("" :: "v"(ct));
    wait_flags<32>(k, lane < 24 ? C_PROG + (u32)(192 + lane) * 128u : C_INIT + 1024 + (lane & 7) * 128, lane < 24 ? 32u : 1u);
    if (idx < 96) {
        if (ASPLIT + (by >> 1) * 32 < 2048) attnA_run(k, rs, ASPLIT + (by >> 1) * 32 + (bx - 4) * 8 + (by & 1) * 4 + wave, 384, 2048, lane);
        const int kk = (bx - 4) * 24 + by;
        out_helper(p, k, lds, kk & 15, kk >> 4, lane, wave);
    } else {
        const int wid = (idx - 96) * 4 + wave, h = wid & 31;
        if (wid == 127) {
            wait_flags(k, C_PROG + (u32)lane * 256u, 1u);
            wait_flags(k, C_PROG + (u32)lane * 256u + 128u, 1u);
            wait_flags(k, lane < 32 ? C_PROG + (u32)(64 + lane) * 256u : C_KM + (u32)lane * 128u, 1u);
            wait_flags(k, C_INIT + (lane & 7) * 128, 1u);
            if (lane == 0) __hip_atomic_store((gu32*)(k.base + C_CALL), k.tag, RLX_AGENT);
        }
        attnA_run(k, rs, ((by - 24) >> 1) * 32 + (bx - 4) * 8 + (by & 1) * 4 + wave, 128, ASPLIT, lane);
        wait_flags(k, C_PROG + (u32)((lane & 1) * 32 + h) * 256u + 128u, 4u);
        {
            BIn a, b;
            wait_flags(k, C_PROG + (u32)(min(lane, 2) * 32 + h) * 256u, 4u);
            attnB_load(a, rs, wid >> 5, h, lane);
            for (int s = wid >> 5; s < QSEG; s += 8) { attnB_task(k, rs, lds + wave * 4096, a, b, s, h, lane); attnB_task(k, rs, lds + wave * 4096, b, a, s + 4, h, lane); }
        }
        {
            QIn a;
            wait_flags(k, C_PROG + (u32)(min(lane, 2) * 32 + idx - 96) * 256u, 64u);
            half_load(a, rs, QSEG + (wave >> 1), idx - 96, wave & 1, lane);
            for (int s = QSEG + (wave >> 1); s < 64; s += 2) attnB_half(k, rs, lds + wave * 4096, a, s, idx - 96, wave & 1, lane);
        }
    }
}

extern "C" void kernel_launch(void* const* d_in, const int* in_sizes, int n_in, void* d_out, int out_size, void* d_ws, size_t ws_size, hipStream_t stream) {
    Params p;
    p.x = (const u16*)d_in[0]; p.wqkv = (const u16*)d_in[1]; p.wo = (const u16*)d_in[2]; p.mem0 = (const u16*)d_in[4];
    p.Wq = (const u16*)d_in[5]; p.Wk = (const u16*)d_in[6]; p.Wv = (const u16*)d_in[7]; p.Wo = (const u16*)d_in[8]; p.gw = (const u16*)d_in[10];
    p.out = (u16*)d_out;
    p.ws = (char*)(((uintptr_t)d_ws + (2u << 20) - 1) & ~(uintptr_t)((2u << 20) - 1));
    (void)hipFuncSetAttribute((const void*)main_kernel, hipFuncAttributeMaxDynamicSharedMemorySize, LDS_BYTES);
    main_kernel<<<256, 256, LDS_BYTES, stream>>>(p);
}
```
